```python
import torch

def _softmax(x, axis=-1):
    return torch.softmax(x, dim=axis)
def _gelu(x):
    return torch.nn.functional.gelu(x, approximate="tanh")
def _silu(x):
    return torch.nn.functional.silu(x)
def _sigmoid(x):
    return torch.sigmoid(x)
def _layernorm(x, axis=-1, eps=1e-5):
    m = x.mean(dim=axis, keepdim=True)
    v = x.var(dim=axis, keepdim=True, unbiased=False)
    return (x - m) / torch.sqrt(v + eps)
def _scatter_add(tgt, idx, src, axis=0):
    return tgt.clone().index_add_(axis, idx, src)
def _segment_sum(src, seg, n):
    out = torch.zeros((n,) + src.shape[1:], dtype=src.dtype)
    return out.index_add_(0, seg, src)
def _pad(x, pw):
    flat = tuple(v for pair in reversed(pw) for v in pair)
    return torch.nn.functional.pad(x, flat)

kernel_name = 'gated_mix__mha_block__mixer_s_mlp__prompt'

def setup_inputs():

    _w = (lambda a: (lambda t: t.to(torch.bfloat16) if t.is_floating_point() else t)(torch.as_tensor(a)))
    def _A_setup():
        B, S, D, H = 1, 2048, 512, 32
        return {
            "x": _w((lambda *s: torch.randn(*s, dtype=torch.bfloat16) * 0.05)(B, S, D)),
            "wqkv": _w((lambda *s: torch.randn(*s, dtype=torch.bfloat16) * 1.6)(D, 3 * D)),
            "wo": _w((lambda *s: torch.randn(*s, dtype=torch.bfloat16) * 0.1)(D, D)),
        }
    _A_in = _A_setup()

    def _B_setup():
        B, S, D, F = 1, 2048, 512, 1792
        return {
            "x":  _w((lambda *s: torch.randn(*s, dtype=torch.bfloat16) * 0.05)(B, S, D)),
            "w1": _w((lambda *s: torch.randn(*s, dtype=torch.bfloat16) * 3.2)(D, D) * (D ** -0.5)),
            "w2": _w((lambda *s: torch.randn(*s, dtype=torch.bfloat16) * 3.2)(D, D) * (D ** -0.5)),
            "w3": _w((lambda *s: torch.randn(*s, dtype=torch.bfloat16) * 3.2)(D, F) * (D ** -0.5)),
            "w4": _w((lambda *s: torch.randn(*s, dtype=torch.bfloat16) * 1.6)(F, D) * (F ** -0.5)),
        }
    _B_in = _B_setup()

    return {
        "A_x": _A_in["x"],
        "A_wqkv": _A_in["wqkv"],
        "A_wo": _A_in["wo"],
        "B_x": _B_in["x"],
        "B_w1": _B_in["w1"],
        "B_w2": _B_in["w2"],
        "B_w3": _B_in["w3"],
        "B_w4": _B_in["w4"],
        "gate_w": _w((lambda *s: torch.randn(*s, dtype=torch.bfloat16) * 0.05)(512) * 16.0),
    }

def reference(A_x, A_wqkv, A_wo, B_x, B_w1, B_w2, B_w3, B_w4, gate_w):
    def _A_ref(x, wqkv, wo):
        B, S, D = x.shape
        H = 32
        Dh = D // H
        qkv = (lambda x, *d: x.permute(*d))((x @ wqkv).reshape(B, S, 3, H, Dh), 2, 0, 3, 1, 4)
        q, k, v = qkv[0], qkv[1], qkv[2]
        scores = torch.einsum("bhqd,bhkd->bhqk", q, k) * (Dh ** -0.5)
        p = _softmax(scores, axis=-1)
        o = torch.einsum("bhqk,bhkd->bhqd", p, v)
        return (lambda x, *d: x.permute(*d))(o, 0, 2, 1, 3).reshape(B, S, D) @ wo

    def _B_ref(x, w1, w2, w3, w4):
        B, S, D = x.shape
        h = _gelu(_layernorm(x, axis=-1) @ w1)
        c = D // 4
        g0 = _pad(h[:, :, 0*c:1*c], ((0, 0), (1, 0), (0, 0)))[:, :S, :]
        g1 = _pad(h[:, :, 1*c:2*c], ((0, 0), (0, 1), (0, 0)))[:, 1:, :]
        g2 = _pad(h[:, :, 2*c:3*c], ((0, 0), (2, 0), (0, 0)))[:, :S, :]
        g3 = _pad(h[:, :, 3*c:   ], ((0, 0), (0, 2), (0, 0)))[:, 2:, :]
        hs = torch.concatenate([g0, g1, g2, g3], axis=-1) @ w2
        x1 = x + hs
        h2 = _gelu(_layernorm(x1, axis=-1) @ w3) @ w4
        return x1 + h2

    x = A_x
    g = _sigmoid((x * gate_w).sum(-1, keepdims=True))
    a = _A_ref(x, A_wqkv, A_wo)
    b = _B_ref(x, B_w1, B_w2, B_w3, B_w4)
    return g * a + (1 - g) * b
```

```cpp
#include <hip/hip_runtime.h>
#include <hip/hip_bf16.h>

typedef unsigned short u16;
typedef __bf16 bf8 __attribute__((ext_vector_type(8)));
typedef __bf16 bf2 __attribute__((ext_vector_type(2)));
typedef float f16v __attribute__((ext_vector_type(16)));
typedef float f4 __attribute__((ext_vector_type(4)));
typedef float f2 __attribute__((ext_vector_type(2)));
typedef unsigned u4 __attribute__((ext_vector_type(4)));
typedef unsigned u2 __attribute__((ext_vector_type(2)));
typedef short s4 __attribute__((ext_vector_type(4)));
#define LAS __attribute__((address_space(3)))
#define GAS __attribute__((address_space(1)))
#define DI __device__ __forceinline__

#define MB (1u << 20)
#define O_QP (0u)
#define O_KP (2u * MB)
#define O_VP (4u * MB)
#define O_HS (6u * MB)
#define O_X1 (8u * MB)
#define O_BP (10u * MB)
#define O_OP (12u * MB)
#define O_H2 (14u * MB)
#define O_W2 (21u * MB)
#define O_WO (21u * MB + 512u * 1024u)
#define O_W3 (22u * MB)
#define O_W4 (24u * MB)
#define O_ST (26u * MB)
#define O_CS (27u * MB)
#define O_G (27u * MB + 8192u)
#define O_KM (27u * MB + 16384u)
#define O_CN (27u * MB + 32768u)

DI f16v mfma(u4 a, u4 b, f16v c) {
  return __builtin_amdgcn_mfma_f32_32x32x16_bf16(__builtin_bit_cast(bf8, a), __builtin_bit_cast(bf8, b), c, 0, 0, 0);
}
DI unsigned pk(float a, float b) {
  f2 v = {a, b};
  return __builtin_bit_cast(unsigned, __builtin_convertvector(v, bf2));
}
DI float dot2(unsigned a, unsigned b, float c) {
  return __builtin_amdgcn_fdot2_f32_bf16(__builtin_bit_cast(bf2, a), __builtin_bit_cast(bf2, b), c, false);
}
DI float blo(unsigned v) { return __builtin_bit_cast(float, v << 16); }
DI float bhi(unsigned v) { return __builtin_bit_cast(float, v & 0xffff0000u); }
DI float gelu(float v) {
  float u = v * (0.7978845608f + 0.0356774081f * v * v);
  float e = __builtin_amdgcn_exp2f(-2.8853900818f * u);
  return v * __builtin_amdgcn_rcpf(1.0f + e);
}
DI float xor32(float v) { return __shfl_xor(v, 32); }
DI void glds(const void* g, char* l) {
  __builtin_amdgcn_global_load_lds((const GAS unsigned*)g, (LAS unsigned*)l, 16, 0, 0);
}
#define WAITV(n) asm volatile("s_waitcnt vmcnt(" #n ")" ::: "memory")
#define BAR() do { asm volatile("s_waitcnt lgkmcnt(0)" ::: "memory"); __builtin_amdgcn_s_barrier(); asm volatile("" ::: "memory"); } while (0)
#define ONES 0x3f803f80u

template <int MODE>
DI void k1_gemm(char* sm, const u16* X, const u16* W, int ldw, int nb, int tb, const u16* gate, char* ws) {
  const int tid = threadIdx.x, lane = tid & 63, wid = __builtin_amdgcn_readfirstlane(tid >> 6);
  const int wr = wid >> 1, wc = wid & 1, h = lane >> 5, l31 = lane & 31;
  const u16* xs = X + (32 * wid + (lane >> 2)) * 512 + (((lane & 3) ^ ((lane >> 4) & 3)) * 8);
  const u16* wsA = W + (8 * wid + (lane >> 4)) * ldw + (((lane & 15) ^ (((lane >> 4) << 2) | ((2 * wid) & 3))) * 8);
  const u16* wsB = W + (8 * wid + 4 + (lane >> 4)) * ldw + (((lane & 15) ^ (((lane >> 4) << 2) | ((2 * wid + 1) & 3))) * 8);
  char* xl = sm + wid * 2048;
  char* wl = sm + 8192 + wid * 2048;
  if (MODE == 0) {
    if (tid < 64) *(u4*)(sm + 65536 + tid * 16) = *(const u4*)(gate + tid * 8);
  }
#define STAGE(kc) do { glds(xs + (kc) * 32, xl + ((kc) & 3) * 16384); glds(xs + 16 * 512 + (kc) * 32, xl + ((kc) & 3) * 16384 + 1024); \
    glds(wsA + (kc) * 32 * ldw, wl + ((kc) & 3) * 16384); glds(wsB + (kc) * 32 * ldw, wl + ((kc) & 3) * 16384 + 1024); } while (0)
  STAGE(0); STAGE(1); STAGE(2);
  const int g1 = (lane >> 4) & 1, q = (lane & 15) >> 2, p = lane & 3;
  int aoff[2][2], boff[2];
#pragma unroll
  for (int nt = 0; nt < 2; nt++)
#pragma unroll
    for (int e = 0; e < 2; e++)
      aoff[nt][e] = 8192 + 256 * (8 * h + 4 * e + q) + 16 * ((8 * wr + 4 * nt + 2 * g1 + (p >> 1)) ^ ((q << 2) | (2 * h + e))) + 8 * (p & 1);
#pragma unroll
  for (int s = 0; s < 2; s++) boff[s] = (64 * wc + l31) * 64 + (((2 * s + h) ^ ((l31 >> 2) & 3)) * 16);
  f16v acc[2][2];
#pragma unroll
  for (int i = 0; i < 16; i++) { acc[0][0][i] = 0.f; acc[0][1][i] = 0.f; acc[1][0][i] = 0.f; acc[1][1][i] = 0.f; }
  float s1[2] = {0.f, 0.f}, s2[2] = {0.f, 0.f}, cs[2] = {0.f, 0.f};
#pragma unroll
  for (int kc = 0; kc < 16; kc++) {
    if (kc < 14) WAITV(8); else if (kc == 14) WAITV(4); else WAITV(0);
    BAR();
    if (kc + 3 < 16) STAGE(kc + 3);
    const char* sb = sm + (kc & 3) * 16384;
#pragma unroll
    for (int s = 0; s < 2; s++) {
      u4 a[2], b[2];
#pragma unroll
      for (int nt = 0; nt < 2; nt++) {
        s4 lo = __builtin_amdgcn_ds_read_tr16_b64_v4i16((LAS s4*)(sb + aoff[nt][0] + s * 4096));
        s4 hi = __builtin_amdgcn_ds_read_tr16_b64_v4i16((LAS s4*)(sb + aoff[nt][1] + s * 4096));
        u2 l2 = __builtin_bit_cast(u2, lo), h2 = __builtin_bit_cast(u2, hi);
        a[nt] = (u4){l2[0], l2[1], h2[0], h2[1]};
        b[nt] = *(const u4*)(sb + boff[s] + nt * 2048);
      }
#pragma unroll
      for (int nt = 0; nt < 2; nt++)
#pragma unroll
        for (int tt = 0; tt < 2; tt++)
          acc[nt][tt] = (MODE == 2) ? mfma(b[tt], a[nt], acc[nt][tt]) : mfma(a[nt], b[tt], acc[nt][tt]);
      if (MODE == 3) {
        if (s == wr) {
#pragma unroll
          for (int tt = 0; tt < 2; tt++)
#pragma unroll
            for (int i = 0; i < 4; i++) { s1[tt] = dot2(b[tt][i], ONES, s1[tt]); s2[tt] = dot2(b[tt][i], b[tt][i], s2[tt]); }
        }
        if (s == wc) {
#pragma unroll
          for (int nt = 0; nt < 2; nt++)
#pragma unroll
            for (int i = 0; i < 4; i++) cs[nt] = dot2(a[nt][i], ONES, cs[nt]);
        }
      }
      if (MODE == 0) {
        if (s == wr) {
          u4 gw = *(const u4*)(sm + 65536 + (kc * 32 + s * 16 + h * 8) * 2);
#pragma unroll
          for (int tt = 0; tt < 2; tt++)
#pragma unroll
            for (int i = 0; i < 4; i++) s1[tt] = dot2(b[tt][i], gw[i], s1[tt]);
        }
      }
    }
  }
#undef STAGE
  const int T0 = tb * 4 + wc * 2;
  if (MODE == 0 || MODE == 1) {
    u4* dst = (u4*)(ws + (MODE == 0 ? O_QP : O_KP));
    const float sc = MODE == 0 ? 0.25f * 1.4426950409f : 1.0f;
#pragma unroll
    for (int nt = 0; nt < 2; nt++)
#pragma unroll
      for (int hh = 0; hh < 2; hh++) {
        const int head = (nb & 3) * 8 + wr * 4 + nt * 2 + hh;
        float mx = 0.f;
#pragma unroll
        for (int tt = 0; tt < 2; tt++) {
          u4 v;
#pragma unroll
          for (int i = 0; i < 4; i++) v[i] = pk(acc[nt][tt][8 * hh + 2 * i] * sc, acc[nt][tt][8 * hh + 2 * i + 1] * sc);
          dst[(head * 64 + T0 + tt) * 64 + lane] = v;
          if (MODE == 1) {
            float n2 = dot2(v[0], v[0], dot2(v[1], v[1], dot2(v[2], v[2], dot2(v[3], v[3], 0.f))));
            mx = fmaxf(mx, n2 + xor32(n2));
          }
        }
        if (MODE == 1) {
#pragma unroll
          for (int o = 16; o >= 1; o >>= 1) mx = fmaxf(mx, __shfl_xor(mx, o));
          if (lane == 0) ((float*)(ws + O_KM))[head * 32 + tb * 2 + wc] = mx;
        }
      }
    if (MODE == 0 && nb == 0) {
      float* red = (float*)sm;
      BAR();
      s1[0] += xor32(s1[0]); s1[1] += xor32(s1[1]);
      red[wr * 128 + wc * 64 + lane] = h ? s1[1] : s1[0];
      BAR();
      if (tid < 128) {
        float z = red[tid] + red[128 + tid];
        ((float*)(ws + O_G))[tb * 128 + tid] = __builtin_amdgcn_rcpf(1.0f + __builtin_amdgcn_exp2f(-1.4426950409f * z));
      }
    }
  } else if (MODE == 2) {
    u4* dst = (u4*)(ws + O_VP);
#pragma unroll
    for (int nt = 0; nt < 2; nt++) {
      const int head = (nb & 3) * 8 + wr * 4 + nt * 2 + g1;
#pragma unroll
      for (int tt = 0; tt < 2; tt++)
#pragma unroll
        for (int s = 0; s < 2; s++) {
          u4 v;
#pragma unroll
          for (int i = 0; i < 4; i++) v[i] = pk(acc[nt][tt][8 * s + 2 * i], acc[nt][tt][8 * s + 2 * i + 1]);
          dst[(head * 64 + T0 + tt) * 64 + s * 32 + h * 16 + (lane & 15)] = v;
        }
    }
  } else {
    float* red = (float*)sm;
    BAR();
#pragma unroll
    for (int i = 0; i < 2; i++) { s1[i] += xor32(s1[i]); s2[i] += xor32(s2[i]); cs[i] += xor32(cs[i]); }
    red[wr * 128 + wc * 64 + lane] = h ? s1[1] : s1[0];
    red[256 + wr * 128 + wc * 64 + lane] = h ? s2[1] : s2[0];
    red[512 + wc * 128 + wr * 64 + lane] = h ? cs[1] : cs[0];
    BAR();
    const int grp = nb & 3;
    const int dl = grp == 0 ? 1 : grp == 1 ? -1 : grp == 2 ? 2 : -2;
    u4* dst = (u4*)(ws + O_HS);
#pragma unroll
    for (int tt = 0; tt < 2; tt++) {
      const int tl = wc * 64 + tt * 32 + l31;
      const float mean = (red[tl] + red[128 + tl]) * (1.f / 512.f);
      const float var = fmaxf((red[256 + tl] + red[384 + tl]) * (1.f / 512.f) - mean * mean, 0.f);
      const float rstd = __builtin_amdgcn_rsqf(var + 1e-5f);
      const int td = tb * 128 + tl + dl;
      const bool ok = (unsigned)td < 2048u;
      const int tw = td & 2047;
#pragma unroll
      for (int nt = 0; nt < 2; nt++)
#pragma unroll
        for (int s = 0; s < 2; s++) {
          u4 v;
#pragma unroll
          for (int jj = 0; jj < 2; jj++) {
            const int f = wr * 64 + nt * 32 + 16 * s + 8 * jj + 4 * h;
            f4 c = *(const f4*)(red + 512 + f) + *(const f4*)(red + 640 + f);
            float r[4];
#pragma unroll
            for (int i = 0; i < 4; i++) r[i] = gelu(rstd * (acc[nt][tt][8 * s + 4 * jj + i] - mean * c[i]));
            v[2 * jj] = ok ? pk(r[0], r[1]) : 0u;
            v[2 * jj + 1] = ok ? pk(r[2], r[3]) : 0u;
          }
          dst[((tw >> 5) * 32 + grp * 8 + wr * 4 + nt * 2 + s) * 64 + h * 32 + (tw & 31)] = v;
        }
    }
  }
}

__global__ __launch_bounds__(256, 2) void k1(const u16* x, const u16* wqkv, const u16* wo, const u16* w1, const u16* w2,
                                             const u16* w3, const u16* w4, const u16* gate, char* ws) {
  __shared__ __attribute__((aligned(16))) char sm[65536 + 1024];
  const int b = blockIdx.x, tid = threadIdx.x;
  if (b < 256) {
    const int xcd = b & 7, i = b >> 3;
    const int md = i & 3, nn = md * 4 + (xcd & 3), tb = (xcd >> 2) * 8 + (i >> 2);
    const u16* X = x + tb * 128 * 512;
    if (md == 0) k1_gemm<0>(sm, X, wqkv + nn * 128, 1536, nn, tb, gate, ws);
    else if (md == 1) k1_gemm<1>(sm, X, wqkv + nn * 128, 1536, nn, tb, gate, ws);
    else if (md == 2) k1_gemm<2>(sm, X, wqkv + nn * 128, 1536, nn, tb, gate, ws);
    else k1_gemm<3>(sm, X, w1 + (nn - 12) * 128, 512, nn, tb, gate, ws);
  } else if (b < 256 + 288) {
    int u = (b - 256) * 256 + tid;
    if (b == 256 && tid < 64) ((unsigned*)(ws + O_CN))[tid * 32] = 0u;
    const u16* src; u4* dst; int N, KS;
    if (u < 8192) { src = w2; dst = (u4*)(ws + O_W2); N = 512; KS = 32; }
    else if (u < 16384) { u -= 8192; src = wo; dst = (u4*)(ws + O_WO); N = 512; KS = 32; }
    else if (u < 45056) { u -= 16384; src = w3; dst = (u4*)(ws + O_W3); N = 1792; KS = 32; }
    else { u -= 45056; src = w4; dst = (u4*)(ws + O_W4); N = 512; KS = 112; }
    const int nq = N >> 2, r = u / nq, n0 = (u - r * nq) * 4, h = r & 1, ks = r >> 1;
    u2 v[8];
#pragma unroll
    for (int j = 0; j < 8; j++) v[j] = *(const u2*)(src + (16 * ks + 8 * (j >> 2) + 4 * h + (j & 3)) * N + n0);
    u4* d = dst + ((n0 >> 5) * KS + ks) * 64 + h * 32 + (n0 & 31);
#pragma unroll
    for (int c = 0; c < 4; c++) {
      u4 o;
#pragma unroll
      for (int dd = 0; dd < 4; dd++)
        o[dd] = __builtin_amdgcn_perm(v[2 * dd + 1][c >> 1], v[2 * dd][c >> 1], (c & 1) ? 0x07060302u : 0x05040100u);
      d[c] = o;
    }
  } else {
    const int cb = b - 544, cq = tid & 15, sl = tid >> 4;
    const u16* src = w3 + (sl * 32) * 1792 + cb * 64 + cq * 4;
    f4 a = {0.f, 0.f, 0.f, 0.f};
#pragma unroll
    for (int r = 0; r < 32; r++) {
      u2 v = *(const u2*)(src + r * 1792);
      a[0] += blo(v[0]); a[1] += bhi(v[0]); a[2] += blo(v[1]); a[3] += bhi(v[1]);
    }
    *(f4*)(sm + (sl * 64 + cq * 4) * 4) = a;
    __syncthreads();
    if (tid < 64) {
      float t = 0.f;
#pragma unroll
      for (int r = 0; r < 16; r++) t += ((const float*)sm)[r * 64 + tid];
      ((float*)(ws + O_CS))[cb * 64 + tid] = t;
    }
  }
}

template <int MT, int NT, int D>
DI void wgemm(f16v (&acc)[MT][NT], const u4* A, int sa, const u4* B, int sb, int nks) {
  u4 a[D][MT], b[D][NT];
#pragma unroll
  for (int d = 0; d < D; d++) {
#pragma unroll
    for (int m = 0; m < MT; m++) a[d][m] = A[m * sa + d * 64];
#pragma unroll
    for (int n = 0; n < NT; n++) b[d][n] = B[n * sb + d * 64];
  }
#pragma unroll 1
  for (int kb = D; kb < nks; kb += D) {
    A += D * 64; B += D * 64;
#pragma unroll
    for (int d = 0; d < D; d++) {
#pragma unroll
      for (int m = 0; m < MT; m++)
#pragma unroll
        for (int n = 0; n < NT; n++) acc[m][n] = mfma(a[d][m], b[d][n], acc[m][n]);
#pragma unroll
      for (int m = 0; m < MT; m++) a[d][m] = A[m * sa + d * 64];
#pragma unroll
      for (int n = 0; n < NT; n++) b[d][n] = B[n * sb + d * 64];
    }
  }
#pragma unroll
  for (int d = 0; d < D; d++)
#pragma unroll
    for (int m = 0; m < MT; m++)
#pragma unroll
      for (int n = 0; n < NT; n++) acc[m][n] = mfma(a[d][m], b[d][n], acc[m][n]);
}
DI unsigned lds_add(unsigned* c) { return __hip_atomic_fetch_add(c, 1u, __ATOMIC_RELAXED, __HIP_MEMORY_SCOPE_WORKGROUP); }
DI void lds_arrive(unsigned* c, int lane) {
  asm volatile("s_waitcnt vmcnt(0) lgkmcnt(0)" ::: "memory");
  if (lane == 0) lds_add(c);
}
DI void lds_wait(unsigned* c, unsigned n) {
  while (__hip_atomic_load(c, __ATOMIC_RELAXED, __HIP_MEMORY_SCOPE_WORKGROUP) < n) __builtin_amdgcn_s_sleep(1);
  asm volatile("" ::: "memory");
}
DI void publish(unsigned* lc, GAS unsigned* gc, int lane) {
  asm volatile("s_waitcnt vmcnt(0)" ::: "memory");
  if (lane == 0) {
    if (lds_add(lc) == 3u) __hip_atomic_fetch_add(gc, 1u, __ATOMIC_RELAXED, __HIP_MEMORY_SCOPE_AGENT);
  }
}
DI void consume(GAS unsigned* gc, unsigned n) {
  while (__hip_atomic_load(gc, __ATOMIC_RELAXED, __HIP_MEMORY_SCOPE_AGENT) < n) __builtin_amdgcn_s_sleep(2);
  __builtin_amdgcn_fence(__ATOMIC_ACQUIRE, "agent");
}
#define L_V 65536
#define L_C 131072
#define L_R 133120
#define L_N 149504

__global__ __launch_bounds__(768, 3) void k2(const u16* x, char* ws) {
  __shared__ __attribute__((aligned(16))) char sm[L_N + 64];
  const int b = blockIdx.x, tid = threadIdx.x, lane = tid & 63, wid = __builtin_amdgcn_readfirstlane(tid >> 6);
  const int h = lane >> 5, l31 = lane & 31;
  const int grp = (b & 7) * 4 + ((b >> 3) & 3), sub = b >> 5;
  unsigned* cn = (unsigned*)(sm + L_N);
  const int qt = sub * 8 + wid;
  u4 qf; float km;
  if (wid < 8) {
    qf = ((const u4*)(ws + O_QP))[(grp * 64 + qt) * 64 + lane];
    km = ((const float*)(ws + O_KM))[grp * 32 + l31];
    asm volatile("" ::: "memory");
    const char* kg = ws + O_KP + grp * 65536 + wid * 2048 + lane * 16;
#pragma unroll
    for (int p = 0; p < 4; p++) {
      glds(kg + p * 16384, sm + wid * 2048 + p * 16384);
      glds(kg + p * 16384 + 1024, sm + wid * 2048 + p * 16384 + 1024);
      glds(kg + 2 * MB + p * 16384, sm + L_V + wid * 2048 + p * 16384);
      glds(kg + 2 * MB + p * 16384 + 1024, sm + L_V + wid * 2048 + p * 16384 + 1024);
    }
  } else {
    const int t = tid - 512;
    if (t < 128) { const unsigned v = (t & 31) == 0 ? ONES : 0u; *(u4*)(sm + L_C + t * 16) = (u4){v, v, v, v}; }
    if (t < 16) cn[t] = 0u;
  }
  BAR();
  if (wid < 8) {
#pragma unroll
    for (int o = 16; o >= 1; o >>= 1) km = fmaxf(km, __shfl_xor(km, o));
    float qn = dot2(qf[0], qf[0], dot2(qf[1], qf[1], dot2(qf[2], qf[2], dot2(qf[3], qf[3], 0.f))));
    qn += xor32(qn);
    float m = __builtin_sqrtf(qn * km) * 1.001f;
    const bool real = l31 < 16;
    const int vst = real ? 2048 : 0;
    const int v0 = real ? L_V + h * 256 + l31 * 16 : L_C + ((l31 & 3) == 0 && l31 < 24 ? 0 : 16);
    asm volatile("s_waitcnt vmcnt(12)" ::: "memory");
    if (lane == 0) lds_add(cn);
    lds_wait(cn, 8);
    f16v o;
#pragma unroll 1
    for (int att = 0; att < 2; att++) {
      f16v ci, sA, sB;
#pragma unroll
      for (int i = 0; i < 16; i++) { ci[i] = -m; o[i] = 0.f; }
      const char* kp = sm + lane * 16;
      const char* vp = sm + v0;
      sA = mfma(*(const u4*)kp, qf, ci);
      u4 k1f = *(const u4*)(kp + 1024);
#pragma unroll 1
      for (int t = 0; t < 64; t += 2) {
        if (t == 14 && att == 0) { lds_arrive(cn + 1, lane); lds_wait(cn + 1, 8); }
        u4 va = *(const u4*)vp, vb = *(const u4*)(vp + 512), vc = *(const u4*)(vp + 1024), vd = *(const u4*)(vp + 1536);
        u4 k2f = *(const u4*)(kp + 2048);
        sB = mfma(k1f, qf, ci);
        k1f = *(const u4*)(kp + 3072);
        u4 p0, p1;
#pragma unroll
        for (int i = 0; i < 4; i++) p0[i] = pk(__builtin_amdgcn_exp2f(sA[2 * i]), __builtin_amdgcn_exp2f(sA[2 * i + 1]));
        o = mfma(va, p0, o);
#pragma unroll
        for (int i = 0; i < 4; i++) p1[i] = pk(__builtin_amdgcn_exp2f(sA[8 + 2 * i]), __builtin_amdgcn_exp2f(sA[9 + 2 * i]));
        o = mfma(vb, p1, o);
        sA = mfma(k2f, qf, ci);
#pragma unroll
        for (int i = 0; i < 4; i++) p0[i] = pk(__builtin_amdgcn_exp2f(sB[2 * i]), __builtin_amdgcn_exp2f(sB[2 * i + 1]));
        o = mfma(vc, p0, o);
#pragma unroll
        for (int i = 0; i < 4; i++) p1[i] = pk(__builtin_amdgcn_exp2f(sB[8 + 2 * i]), __builtin_amdgcn_exp2f(sB[9 + 2 * i]));
        o = mfma(vd, p1, o);
        kp += 2048; vp += vst;
      }
      if (att == 1 || __all(o[8] >= 8.67e-19f && o[8] < 1e30f)) break;
      f16v z;
#pragma unroll
      for (int i = 0; i < 16; i++) z[i] = 0.f;
      float mx = -3.0e38f;
#pragma unroll 1
      for (int t = 0; t < 64; t++) {
        f16v s = mfma(*(const u4*)(sm + lane * 16 + t * 1024), qf, z);
#pragma unroll
        for (int i = 0; i < 16; i++) mx = fmaxf(mx, s[i]);
      }
      m = fmaxf(mx, xor32(mx));
    }
    const float il = 1.0f / o[8];
    u4 r;
#pragma unroll
    for (int i = 0; i < 4; i++) r[i] = pk(o[2 * i] * il, o[2 * i + 1] * il);
    ((u4*)(ws + O_OP))[(qt * 32 + grp) * 64 + lane] = r;
  } else {
    __builtin_amdgcn_s_setprio(3);
    const int gw = wid - 8, mi = gw >> 1, ni = gw & 1;
    GAS unsigned* gc = (GAS unsigned*)(ws + O_CN) + grp * 64;
    const __amdgpu_buffer_rsrc_t rs = __builtin_amdgcn_make_buffer_rsrc((void*)ws, (short)0, 0x7fffffff, 0x00020000);
    const int ft = sub * 2 + mi, tt = grp * 2 + ni;
    {
      f16v acc[1][1];
      u2 xr[4];
#pragma unroll
      for (int q = 0; q < 4; q++) xr[q] = *(const u2*)(x + (tt * 32 + l31) * 512 + ft * 32 + 8 * q + 4 * h);
#pragma unroll
      for (int i = 0; i < 16; i++) acc[0][0][i] = 0.f;
      wgemm<1, 1, 8>(acc, (const u4*)(ws + O_W2) + ft * 2048 + lane, 0, (const u4*)(ws + O_HS) + tt * 2048 + lane, 0, 32);
      float s1 = 0.f, s2 = 0.f;
#pragma unroll
      for (int s = 0; s < 2; s++) {
        u4 v;
#pragma unroll
        for (int i = 0; i < 4; i++) {
          const unsigned xv = xr[2 * s + (i >> 1)][i & 1];
          v[i] = pk(acc[0][0][8 * s + 2 * i] + blo(xv), acc[0][0][8 * s + 2 * i + 1] + bhi(xv));
          s1 = dot2(v[i], ONES, s1); s2 = dot2(v[i], v[i], s2);
        }
        __builtin_amdgcn_raw_buffer_store_b128(v, rs, O_X1 + ((tt * 32 + ft * 2 + s) * 64 + lane) * 16, 0, 16);
      }
      u2 st = {__builtin_bit_cast(unsigned, s1), __builtin_bit_cast(unsigned, s2)};
      __builtin_amdgcn_raw_buffer_store_b64(st, rs, O_ST + ((tt * 16 + ft) * 64 + lane) * 8, 0, 16);
      publish(cn + 2, gc, lane);
    }
    const int j = sub * 4 + gw;
    if (j < 28) {
      f16v acc[2][2];
#pragma unroll
      for (int i = 0; i < 16; i++) { acc[0][0][i] = 0.f; acc[0][1][i] = 0.f; acc[1][0][i] = 0.f; acc[1][1][i] = 0.f; }
      consume(gc, 8);
      float mean[2], rstd[2];
#pragma unroll
      for (int n = 0; n < 2; n++) {
        f2 t = {0.f, 0.f};
#pragma unroll
        for (int f = 0; f < 16; f++) t += ((const f2*)(ws + O_ST))[((grp * 2 + n) * 16 + f) * 64 + lane];
        t[0] += xor32(t[0]); t[1] += xor32(t[1]);
        mean[n] = t[0] * (1.f / 512.f);
        rstd[n] = __builtin_amdgcn_rsqf(fmaxf(t[1] * (1.f / 512.f) - mean[n] * mean[n], 0.f) + 1e-5f);
      }
      wgemm<2, 2, 4>(acc, (const u4*)(ws + O_W3) + j * 4096 + lane, 2048, (const u4*)(ws + O_X1) + grp * 4096 + lane, 2048, 32);
#pragma unroll
      for (int mm = 0; mm < 2; mm++)
#pragma unroll
        for (int s = 0; s < 2; s++) {
          const float* cp = (const float*)(ws + O_CS) + j * 64 + mm * 32 + 16 * s + 4 * h;
          const f4 c0 = *(const f4*)cp, c1 = *(const f4*)(cp + 8);
#pragma unroll
          for (int n = 0; n < 2; n++) {
            float r[8];
#pragma unroll
            for (int i = 0; i < 4; i++) {
              r[i] = gelu(rstd[n] * (acc[mm][n][8 * s + i] - mean[n] * c0[i]));
              r[4 + i] = gelu(rstd[n] * (acc[mm][n][8 * s + 4 + i] - mean[n] * c1[i]));
            }
            u4 v = {pk(r[0], r[1]), pk(r[2], r[3]), pk(r[4], r[5]), pk(r[6], r[7])};
            __builtin_amdgcn_raw_buffer_store_b128(v, rs, O_H2 + (((grp * 2 + n) * 112 + j * 4 + mm * 2 + s) * 64 + lane) * 16, 0, 16);
          }
        }
      publish(cn + 3, gc + 32, lane);
    }
    {
      f16v acc[2][2];
#pragma unroll
      for (int i = 0; i < 16; i++) { acc[0][0][i] = 0.f; acc[0][1][i] = 0.f; acc[1][0][i] = 0.f; acc[1][1][i] = 0.f; }
      consume(gc + 32, 7);
      const u4 x0 = ((const u4*)(ws + O_X1))[(tt * 32 + ft * 2) * 64 + lane], x1 = ((const u4*)(ws + O_X1))[(tt * 32 + ft * 2 + 1) * 64 + lane];
      wgemm<2, 2, 4>(acc, (const u4*)(ws + O_W4) + (ft * 112 + gw * 28) * 64 + lane, (1 - 2 * mi) * 7168,
                     (const u4*)(ws + O_H2) + (tt * 112 + gw * 28) * 64 + lane, (1 - 2 * ni) * 7168, 28);
#pragma unroll
      for (int r = 1; r < 4; r++) {
        f4* dp = (f4*)(sm + L_R + (r ^ gw) * 4096) + lane;
        const f16v a = acc[r >> 1][r & 1];
#pragma unroll
        for (int i = 0; i < 4; i++) dp[i * 64] = (f4){a[4 * i], a[4 * i + 1], a[4 * i + 2], a[4 * i + 3]};
        lds_arrive(cn + 4, lane); lds_wait(cn + 4, 8 * r - 4);
        const f4* sp = (const f4*)(sm + L_R + gw * 4096) + lane;
#pragma unroll
        for (int i = 0; i < 4; i++) {
          const f4 v = sp[i * 64];
          acc[0][0][4 * i] += v[0]; acc[0][0][4 * i + 1] += v[1]; acc[0][0][4 * i + 2] += v[2]; acc[0][0][4 * i + 3] += v[3];
        }
        if (r < 3) { lds_arrive(cn + 4, lane); lds_wait(cn + 4, 8 * r); }
      }
      u4 v, w;
#pragma unroll
      for (int i = 0; i < 4; i++) {
        v[i] = pk(acc[0][0][2 * i] + blo(x0[i]), acc[0][0][2 * i + 1] + bhi(x0[i]));
        w[i] = pk(acc[0][0][8 + 2 * i] + blo(x1[i]), acc[0][0][9 + 2 * i] + bhi(x1[i]));
      }
      ((u4*)(ws + O_BP))[(tt * 32 + ft * 2) * 64 + lane] = v;
      ((u4*)(ws + O_BP))[(tt * 32 + ft * 2 + 1) * 64 + lane] = w;
    }
  }
}

__global__ __launch_bounds__(256, 1) void k3(const char* ws, u16* out) {
  __shared__ __attribute__((aligned(16))) char sm[131072];
  const int b = blockIdx.x, tid = threadIdx.x, lane = tid & 63, wid = __builtin_amdgcn_readfirstlane(tid >> 6);
  const int h = lane >> 5, l31 = lane & 31;
  const int grp = (b & 7) * 4 + ((b >> 3) & 3), sub = b >> 5;
  const int mi = wid >> 1, ni = wid & 1, ft = sub * 2 + mi, tt = grp * 2 + ni;
  const char* src = ws + (wid < 2 ? O_WO + (sub * 2 + wid) * 32768 : O_OP + (grp * 2 + wid - 2) * 32768) + lane * 16;
#pragma unroll
  for (int k = 0; k < 32; k++) glds(src + k * 1024, sm + wid * 32768 + k * 1024);
  f16v acc;
#pragma unroll
  for (int i = 0; i < 16; i++) acc[i] = 0.f;
  const char* ap = sm + mi * 32768 + lane * 16;
  const char* bp = sm + 65536 + ni * 32768 + lane * 16;
  u4 b0, b1; float g;
#pragma unroll
  for (int p = 0; p < 4; p++) {
    if (p == 0) WAITV(24); else if (p == 1) WAITV(16); else if (p == 2) WAITV(8); else WAITV(0);
    BAR();
    if (p == 3) {
      b0 = ((const u4*)(ws + O_BP))[(tt * 32 + ft * 2) * 64 + lane];
      b1 = ((const u4*)(ws + O_BP))[(tt * 32 + ft * 2 + 1) * 64 + lane];
      g = ((const float*)(ws + O_G))[tt * 32 + l31];
    }
#pragma unroll
    for (int k = 0; k < 8; k++) acc = mfma(*(const u4*)(ap + (p * 8 + k) * 1024), *(const u4*)(bp + (p * 8 + k) * 1024), acc);
  }
  const float g1 = 1.0f - g;
#pragma unroll
  for (int s = 0; s < 2; s++) {
    const u4 bb = s ? b1 : b0;
    unsigned r[4];
#pragma unroll
    for (int i = 0; i < 4; i++) r[i] = pk(g * acc[8 * s + 2 * i] + g1 * blo(bb[i]), g * acc[8 * s + 2 * i + 1] + g1 * bhi(bb[i]));
    auto e0 = __builtin_amdgcn_permlane32_swap(r[0], r[2], false, false);
    auto e1 = __builtin_amdgcn_permlane32_swap(r[1], r[3], false, false);
    u4 v = {e0[0], e1[0], e0[1], e1[1]};
    *(u4*)(out + (tt * 32 + l31) * 512 + ft * 32 + 16 * s + 8 * h) = v;
  }
}

extern "C" void kernel_launch(void* const* d_in, const int* in_sizes, int n_in, void* d_out, int out_size, void* d_ws,
                              size_t ws_size, hipStream_t stream) {
  const u16* x = (const u16*)d_in[0];
  char* ws = (char*)d_ws;
  k1<<<256 + 288 + 28, 256, 0, stream>>>(x, (const u16*)d_in[1], (const u16*)d_in[2], (const u16*)d_in[4], (const u16*)d_in[5],
                                         (const u16*)d_in[6], (const u16*)d_in[7], (const u16*)d_in[8], ws);
  k2<<<256, 768, 0, stream>>>(x, ws);
  k3<<<256, 256, 0, stream>>>(ws, (u16*)d_out);
}
```

```cpp
#include <hip/hip_runtime.h>
#include <hip/hip_bf16.h>

typedef unsigned short u16;
typedef __bf16 bf8 __attribute__((ext_vector_type(8)));
typedef __bf16 bf2 __attribute__((ext_vector_type(2)));
typedef float f16v __attribute__((ext_vector_type(16)));
typedef float f4 __attribute__((ext_vector_type(4)));
typedef float f2 __attribute__((ext_vector_type(2)));
typedef unsigned u4 __attribute__((ext_vector_type(4)));
typedef unsigned u2 __attribute__((ext_vector_type(2)));
typedef short s4 __attribute__((ext_vector_type(4)));
#define LAS __attribute__((address_space(3)))
#define GAS __attribute__((address_space(1)))
#define DI __device__ __forceinline__

#define MB (1u << 20)
#define O_QP (0u)
#define O_KP (2u * MB)
#define O_VP (4u * MB)
#define O_HS (6u * MB)
#define O_X1 (8u * MB)
#define O_BP (10u * MB)
#define O_OP (12u * MB)
#define O_H2 (14u * MB)
#define O_W2 (21u * MB)
#define O_WO (21u * MB + 512u * 1024u)
#define O_W3 (22u * MB)
#define O_W4 (24u * MB)
#define O_ST (26u * MB)
#define O_CS (27u * MB)
#define O_G (27u * MB + 8192u)
#define O_KM (27u * MB + 16384u)
#define O_CN (27u * MB + 32768u)

DI f16v mfma(u4 a, u4 b, f16v c) {
  return __builtin_amdgcn_mfma_f32_32x32x16_bf16(__builtin_bit_cast(bf8, a), __builtin_bit_cast(bf8, b), c, 0, 0, 0);
}
DI unsigned pk(float a, float b) {
  f2 v = {a, b};
  return __builtin_bit_cast(unsigned, __builtin_convertvector(v, bf2));
}
DI float dot2(unsigned a, unsigned b, float c) {
  return __builtin_amdgcn_fdot2_f32_bf16(__builtin_bit_cast(bf2, a), __builtin_bit_cast(bf2, b), c, false);
}
DI float blo(unsigned v) { return __builtin_bit_cast(float, v << 16); }
DI float bhi(unsigned v) { return __builtin_bit_cast(float, v & 0xffff0000u); }
DI float gelu(float v) {
  float u = v * (0.7978845608f + 0.0356774081f * v * v);
  float e = __builtin_amdgcn_exp2f(-2.8853900818f * u);
  return v * __builtin_amdgcn_rcpf(1.0f + e);
}
DI float xor32(float v) { return __shfl_xor(v, 32); }
DI void glds(const void* base, unsigned voff, unsigned l) {
  unsigned keep;
  asm volatile("s_mov_b32 %0, m0\n\ts_mov_b32 m0, %3\n\ts_nop 0\n\tglobal_load_lds_dwordx4 %1, %2\n\ts_mov_b32 m0, %0"
               : "=&s"(keep) : "v"(voff), "s"(base), "s"(l) : "memory");
}
DI unsigned laddr(char* p) { return __builtin_amdgcn_readfirstlane((unsigned)(size_t)(LAS char*)p); }
#define WAITV(n) asm volatile("s_waitcnt vmcnt(" #n ")" ::: "memory")
#define BAR() do { asm volatile("s_waitcnt lgkmcnt(0)" ::: "memory"); __builtin_amdgcn_s_barrier(); asm volatile("" ::: "memory"); } while (0)
#define ONES 0x3f803f80u

#define STAMP1(k)
#define STAMP(k)
template <int MODE>
DI void k1_gemm(char* sm, const u16* X, const u16* W, int ldw, int nb, int tb, const u16* gate, char* ws) {
  const int tid = threadIdx.x, lane = tid & 63, wid = __builtin_amdgcn_readfirstlane(tid >> 6);
  const int wr = wid >> 1, wc = wid & 1, h = lane >> 5, l31 = lane & 31;
  const unsigned xs = ((32 * wid + (lane >> 2)) * 512 + (((lane & 3) ^ ((lane >> 4) & 3)) * 8)) * 2;
  const unsigned wsA = ((8 * wid + (lane >> 4)) * ldw + (((lane & 15) ^ (((lane >> 4) << 2) | ((2 * wid) & 3))) * 8)) * 2;
  const unsigned wsB = ((8 * wid + 4 + (lane >> 4)) * ldw + (((lane & 15) ^ (((lane >> 4) << 2) | ((2 * wid + 1) & 3))) * 8)) * 2;
  const unsigned xl = laddr(sm) + wid * 2048, wl = xl + 8192;
  if (MODE == 0) {
    if (tid < 64) *(u4*)(sm + 65536 + tid * 16) = *(const u4*)(gate + tid * 8);
  }
#define STAGE(kc) do { glds(X + (kc) * 32, xs, xl + ((kc) & 3) * 16384); glds(X + 16 * 512 + (kc) * 32, xs, xl + ((kc) & 3) * 16384 + 1024); \
    glds(W + (kc) * 32 * ldw, wsA, wl + ((kc) & 3) * 16384); glds(W + (kc) * 32 * ldw, wsB, wl + ((kc) & 3) * 16384 + 1024); } while (0)
  STAGE(0); STAGE(1); STAGE(2);
  const int g1 = (lane >> 4) & 1, q = (lane & 15) >> 2, p = lane & 3;
  int aoff[2][2], boff[2];
#pragma unroll
  for (int nt = 0; nt < 2; nt++)
#pragma unroll
    for (int e = 0; e < 2; e++)
      aoff[nt][e] = 8192 + 256 * (8 * h + 4 * e + q) + 16 * ((8 * wr + 4 * nt + 2 * g1 + (p >> 1)) ^ ((q << 2) | (2 * h + e))) + 8 * (p & 1);
#pragma unroll
  for (int s = 0; s < 2; s++) boff[s] = (64 * wc + l31) * 64 + (((2 * s + h) ^ ((l31 >> 2) & 3)) * 16);
  f16v acc[2][2];
#pragma unroll
  for (int i = 0; i < 16; i++) { acc[0][0][i] = 0.f; acc[0][1][i] = 0.f; acc[1][0][i] = 0.f; acc[1][1][i] = 0.f; }
  float s1[2] = {0.f, 0.f}, s2[2] = {0.f, 0.f}, cs[2] = {0.f, 0.f};
#pragma unroll
  for (int kc = 0; kc < 16; kc++) {
    if (kc < 14) WAITV(8); else if (kc == 14) WAITV(4); else WAITV(0);
    BAR();
    if (kc == 0) STAMP1(1);
    if (kc + 3 < 16) STAGE(kc + 3);
    const char* sb = sm + (kc & 3) * 16384;
#pragma unroll
    for (int s = 0; s < 2; s++) {
      u4 a[2], b[2];
#pragma unroll
      for (int nt = 0; nt < 2; nt++) {
        s4 lo = __builtin_amdgcn_ds_read_tr16_b64_v4i16((LAS s4*)(sb + aoff[nt][0] + s * 4096));
        s4 hi = __builtin_amdgcn_ds_read_tr16_b64_v4i16((LAS s4*)(sb + aoff[nt][1] + s * 4096));
        u2 l2 = __builtin_bit_cast(u2, lo), h2 = __builtin_bit_cast(u2, hi);
        a[nt] = (u4){l2[0], l2[1], h2[0], h2[1]};
        b[nt] = *(const u4*)(sb + boff[s] + nt * 2048);
      }
#pragma unroll
      for (int nt = 0; nt < 2; nt++)
#pragma unroll
        for (int tt = 0; tt < 2; tt++)
          acc[nt][tt] = (MODE == 2) ? mfma(b[tt], a[nt], acc[nt][tt]) : mfma(a[nt], b[tt], acc[nt][tt]);
      if (MODE == 3) {
        if (s == wr) {
#pragma unroll
          for (int tt = 0; tt < 2; tt++)
#pragma unroll
            for (int i = 0; i < 4; i++) { s1[tt] = dot2(b[tt][i], ONES, s1[tt]); s2[tt] = dot2(b[tt][i], b[tt][i], s2[tt]); }
        }
        if (s == wc) {
#pragma unroll
          for (int nt = 0; nt < 2; nt++)
#pragma unroll
            for (int i = 0; i < 4; i++) cs[nt] = dot2(a[nt][i], ONES, cs[nt]);
        }
      }
      if (MODE == 0) {
        if (s == wr) {
          u4 gw = *(const u4*)(sm + 65536 + (kc * 32 + s * 16 + h * 8) * 2);
#pragma unroll
          for (int tt = 0; tt < 2; tt++)
#pragma unroll
            for (int i = 0; i < 4; i++) s1[tt] = dot2(b[tt][i], gw[i], s1[tt]);
        }
      }
    }
  }
#undef STAGE
  STAMP1(2);
  const int T0 = tb * 4 + wc * 2;
  if (MODE == 0 || MODE == 1) {
    u4* dst = (u4*)(ws + (MODE == 0 ? O_QP : O_KP));
    const float sc = MODE == 0 ? 0.25f * 1.4426950409f : 1.0f;
#pragma unroll
    for (int nt = 0; nt < 2; nt++)
#pragma unroll
      for (int hh = 0; hh < 2; hh++) {
        const int head = (nb & 3) * 8 + wr * 4 + nt * 2 + hh;
        float mx = 0.f;
#pragma unroll
        for (int tt = 0; tt < 2; tt++) {
          u4 v;
#pragma unroll
          for (int i = 0; i < 4; i++) v[i] = pk(acc[nt][tt][8 * hh + 2 * i] * sc, acc[nt][tt][8 * hh + 2 * i + 1] * sc);
          dst[(head * 64 + T0 + tt) * 64 + lane] = v;
          if (MODE == 1) {
            float n2 = dot2(v[0], v[0], dot2(v[1], v[1], dot2(v[2], v[2], dot2(v[3], v[3], 0.f))));
            mx = fmaxf(mx, n2 + xor32(n2));
          }
        }
        if (MODE == 1) {
#pragma unroll
          for (int o = 16; o >= 1; o >>= 1) mx = fmaxf(mx, __shfl_xor(mx, o));
          if (lane == 0) ((float*)(ws + O_KM))[head * 32 + tb * 2 + wc] = mx;
        }
      }
    if (MODE == 0 && nb == 0) {
      float* red = (float*)sm;
      BAR();
      s1[0] += xor32(s1[0]); s1[1] += xor32(s1[1]);
      red[wr * 128 + wc * 64 + lane] = h ? s1[1] : s1[0];
      BAR();
      if (tid < 128) {
        float z = red[tid] + red[128 + tid];
        ((float*)(ws + O_G))[tb * 128 + tid] = __builtin_amdgcn_rcpf(1.0f + __builtin_amdgcn_exp2f(-1.4426950409f * z));
      }
    }
  } else if (MODE == 2) {
    u4* dst = (u4*)(ws + O_VP);
#pragma unroll
    for (int nt = 0; nt < 2; nt++) {
      const int head = (nb & 3) * 8 + wr * 4 + nt * 2 + g1;
#pragma unroll
      for (int tt = 0; tt < 2; tt++)
#pragma unroll
        for (int s = 0; s < 2; s++) {
          u4 v;
#pragma unroll
          for (int i = 0; i < 4; i++) v[i] = pk(acc[nt][tt][8 * s + 2 * i], acc[nt][tt][8 * s + 2 * i + 1]);
          dst[(head * 64 + T0 + tt) * 64 + s * 32 + h * 16 + (lane & 15)] = v;
        }
    }
  } else {
    float* red = (float*)sm;
    BAR();
#pragma unroll
    for (int i = 0; i < 2; i++) { s1[i] += xor32(s1[i]); s2[i] += xor32(s2[i]); cs[i] += xor32(cs[i]); }
    red[wr * 128 + wc * 64 + lane] = h ? s1[1] : s1[0];
    red[256 + wr * 128 + wc * 64 + lane] = h ? s2[1] : s2[0];
    red[512 + wc * 128 + wr * 64 + lane] = h ? cs[1] : cs[0];
    BAR();
    const int grp = nb & 3;
    const int dl = grp == 0 ? 1 : grp == 1 ? -1 : grp == 2 ? 2 : -2;
    u4* dst = (u4*)(ws + O_HS);
#pragma unroll
    for (int tt = 0; tt < 2; tt++) {
      const int tl = wc * 64 + tt * 32 + l31;
      const float mean = (red[tl] + red[128 + tl]) * (1.f / 512.f);
      const float var = fmaxf((red[256 + tl] + red[384 + tl]) * (1.f / 512.f) - mean * mean, 0.f);
      const float rstd = __builtin_amdgcn_rsqf(var + 1e-5f);
      const int td = tb * 128 + tl + dl;
      const bool ok = (unsigned)td < 2048u;
      const int tw = td & 2047;
#pragma unroll
      for (int nt = 0; nt < 2; nt++)
#pragma unroll
        for (int s = 0; s < 2; s++) {
          u4 v;
#pragma unroll
          for (int jj = 0; jj < 2; jj++) {
            const int f = wr * 64 + nt * 32 + 16 * s + 8 * jj + 4 * h;
            f4 c = *(const f4*)(red + 512 + f) + *(const f4*)(red + 640 + f);
            float r[4];
#pragma unroll
            for (int i = 0; i < 4; i++) r[i] = gelu(rstd * (acc[nt][tt][8 * s + 4 * jj + i] - mean * c[i]));
            v[2 * jj] = ok ? pk(r[0], r[1]) : 0u;
            v[2 * jj + 1] = ok ? pk(r[2], r[3]) : 0u;
          }
          dst[((tw >> 5) * 32 + grp * 8 + wr * 4 + nt * 2 + s) * 64 + h * 32 + (tw & 31)] = v;
        }
    }
  }
}

__global__ __launch_bounds__(256, 2) void k1(const u16* x, const u16* wqkv, const u16* wo, const u16* w1, const u16* w2,
                                             const u16* w3, const u16* w4, const u16* gate, char* ws) {
  __shared__ __attribute__((aligned(16))) char sm[65536 + 1024];
  const int b = blockIdx.x, tid = threadIdx.x;
  STAMP1(0);
  if (b < 256) {
    const int xcd = b & 7, i = b >> 3;
    const int md = i & 3, nn = md * 4 + (xcd & 3), tb = (xcd >> 2) * 8 + (i >> 2);
    const u16* X = x + tb * 128 * 512;
    if (md == 0) k1_gemm<0>(sm, X, wqkv + nn * 128, 1536, nn, tb, gate, ws);
    else if (md == 1) k1_gemm<1>(sm, X, wqkv + nn * 128, 1536, nn, tb, gate, ws);
    else if (md == 2) k1_gemm<2>(sm, X, wqkv + nn * 128, 1536, nn, tb, gate, ws);
    else k1_gemm<3>(sm, X, w1 + (nn - 12) * 128, 512, nn, tb, gate, ws);
  } else if (b < 256 + 288) {
    int u = (b - 256) * 256 + tid;
    if (b == 256 && tid < 64) ((unsigned*)(ws + O_CN))[tid * 32] = 0u;
    const u16* src; u4* dst; int N, KS;
    if (u < 8192) { src = w2; dst = (u4*)(ws + O_W2); N = 512; KS = 32; }
    else if (u < 16384) { u -= 8192; src = wo; dst = (u4*)(ws + O_WO); N = 512; KS = 32; }
    else if (u < 45056) { u -= 16384; src = w3; dst = (u4*)(ws + O_W3); N = 1792; KS = 32; }
    else { u -= 45056; src = w4; dst = (u4*)(ws + O_W4); N = 512; KS = 112; }
    const int nq = N >> 2, r = u / nq, n0 = (u - r * nq) * 4, h = r & 1, ks = r >> 1;
    u2 v[8];
#pragma unroll
    for (int j = 0; j < 8; j++) v[j] = *(const u2*)(src + (16 * ks + 8 * (j >> 2) + 4 * h + (j & 3)) * N + n0);
    u4* d = dst + ((n0 >> 5) * KS + ks) * 64 + h * 32 + (n0 & 31);
#pragma unroll
    for (int c = 0; c < 4; c++) {
      u4 o;
#pragma unroll
      for (int dd = 0; dd < 4; dd++)
        o[dd] = __builtin_amdgcn_perm(v[2 * dd + 1][c >> 1], v[2 * dd][c >> 1], (c & 1) ? 0x07060302u : 0x05040100u);
      d[c] = o;
    }
  } else {
    const int cb = b - 544, cq = tid & 15, sl = tid >> 4;
    const u16* src = w3 + (sl * 32) * 1792 + cb * 64 + cq * 4;
    f4 a = {0.f, 0.f, 0.f, 0.f};
#pragma unroll
    for (int r = 0; r < 32; r++) {
      u2 v = *(const u2*)(src + r * 1792);
      a[0] += blo(v[0]); a[1] += bhi(v[0]); a[2] += blo(v[1]); a[3] += bhi(v[1]);
    }
    *(f4*)(sm + (sl * 64 + cq * 4) * 4) = a;
    __syncthreads();
    if (tid < 64) {
      float t = 0.f;
#pragma unroll
      for (int r = 0; r < 16; r++) t += ((const float*)sm)[r * 64 + tid];
      ((float*)(ws + O_CS))[cb * 64 + tid] = t;
    }
  }
  STAMP1(3);
}

typedef __amdgpu_buffer_rsrc_t rsrc_t;
#define BLD(off, so, aux) __builtin_amdgcn_raw_buffer_load_b128(rs, (off), (so), (aux))
#define SB() __builtin_amdgcn_sched_barrier(0)
template <int MT, int NT, int D, int AUX>
DI void wgemm(f16v (&acc)[MT][NT], rsrc_t rs, int A, int sa, int B, int sb, int nks) {
  u4 a[D][MT], b[D][NT];
#pragma unroll
  for (int d = 0; d < D; d++) {
#pragma unroll
    for (int m = 0; m < MT; m++) a[d][m] = BLD(A + m * sa + (d & 3) * 1024, (d >> 2) * 4096, 0);
#pragma unroll
    for (int n = 0; n < NT; n++) b[d][n] = BLD(B + n * sb + (d & 3) * 1024, (d >> 2) * 4096, AUX);
    SB();
  }
#pragma unroll 1
  for (int kb = D; kb < nks; kb += D) {
#pragma unroll
    for (int d = 0; d < D; d++) {
#pragma unroll
      for (int m = 0; m < MT; m++)
#pragma unroll
        for (int n = 0; n < NT; n++) acc[m][n] = mfma(a[d][m], b[d][n], acc[m][n]);
      SB();
#pragma unroll
      for (int m = 0; m < MT; m++) a[d][m] = BLD(A + m * sa + (d & 3) * 1024, kb * 1024 + (d >> 2) * 4096, 0);
#pragma unroll
      for (int n = 0; n < NT; n++) b[d][n] = BLD(B + n * sb + (d & 3) * 1024, kb * 1024 + (d >> 2) * 4096, AUX);
      SB();
    }
  }
#pragma unroll
  for (int d = 0; d < D; d++)
#pragma unroll
    for (int m = 0; m < MT; m++)
#pragma unroll
      for (int n = 0; n < NT; n++) acc[m][n] = mfma(a[d][m], b[d][n], acc[m][n]);
}
DI unsigned lds_add(unsigned* c) { return __hip_atomic_fetch_add(c, 1u, __ATOMIC_RELAXED, __HIP_MEMORY_SCOPE_WORKGROUP); }
DI void lds_arrive(unsigned* c, int lane) {
  asm volatile("s_waitcnt vmcnt(0) lgkmcnt(0)" ::: "memory");
  if (lane == 0) lds_add(c);
}
DI void lds_wait(unsigned* c, unsigned n) {
  while (__hip_atomic_load(c, __ATOMIC_RELAXED, __HIP_MEMORY_SCOPE_WORKGROUP) < n) __builtin_amdgcn_s_sleep(1);
  asm volatile("" ::: "memory");
}
DI void publish(unsigned* lc, GAS unsigned* gc, int lane) {
  asm volatile("s_waitcnt vmcnt(0)" ::: "memory");
  if (lane == 0) {
    if (lds_add(lc) == 3u) __hip_atomic_fetch_add(gc, 1u, __ATOMIC_RELAXED, __HIP_MEMORY_SCOPE_AGENT);
  }
}
DI void consume(GAS unsigned* gc, unsigned n) {
  while (__hip_atomic_load(gc, __ATOMIC_RELAXED, __HIP_MEMORY_SCOPE_AGENT) < n) __builtin_amdgcn_s_sleep(2);
  __builtin_amdgcn_fence(__ATOMIC_ACQUIRE, "wavefront");
  asm volatile("" ::: "memory");
}
#define L_V 65536
#define L_C 131072
#define L_R 133120
#define L_N 149504

__global__ __launch_bounds__(768, 3) void k2(const u16* x, char* ws) {
  __shared__ __attribute__((aligned(16))) char sm[L_N + 64];
  const int b = blockIdx.x, tid = threadIdx.x, lane = tid & 63, wid = __builtin_amdgcn_readfirstlane(tid >> 6);
  const int h = lane >> 5, l31 = lane & 31;
  const int grp = (b & 7) * 4 + ((b >> 3) & 3), sub = b >> 5;
  unsigned* cn = (unsigned*)(sm + L_N);
  const int qt = sub * 8 + wid;
  u4 qf; float km;
  STAMP(0);
  if (wid < 8) {
    qf = ((const u4*)(ws + O_QP))[(grp * 64 + qt) * 64 + lane];
    km = ((const float*)(ws + O_KM))[grp * 32 + l31];
    asm volatile("" ::: "memory");
    const char* kg = ws + O_KP + grp * 65536 + wid * 2048;
    const unsigned lb = laddr(sm) + wid * 2048, vo = lane * 16;
#pragma unroll
    for (int p = 0; p < 4; p++) {
      glds(kg + p * 16384, vo, lb + p * 16384);
      glds(kg + p * 16384 + 1024, vo, lb + p * 16384 + 1024);
      glds(kg + 2 * MB + p * 16384, vo, lb + L_V + p * 16384);
      glds(kg + 2 * MB + p * 16384 + 1024, vo, lb + L_V + p * 16384 + 1024);
    }
  } else {
    const int t = tid - 512;
    if (t < 128) { const unsigned v = (t & 31) == 0 ? ONES : 0u; *(u4*)(sm + L_C + t * 16) = (u4){v, v, v, v}; }
    if (t < 16) cn[t] = 0u;
  }
  BAR();
  if (wid < 8) {
#pragma unroll
    for (int o = 16; o >= 1; o >>= 1) km = fmaxf(km, __shfl_xor(km, o));
    float qn = dot2(qf[0], qf[0], dot2(qf[1], qf[1], dot2(qf[2], qf[2], dot2(qf[3], qf[3], 0.f))));
    qn += xor32(qn);
    float m = __builtin_sqrtf(qn * km) * 1.001f;
    const bool real = l31 < 16;
    const int vst = real ? 2048 : 0;
    const int v0 = real ? L_V + h * 256 + l31 * 16 : L_C + ((l31 & 3) == 0 && l31 < 24 ? 0 : 16);
    asm volatile("s_waitcnt vmcnt(12)" ::: "memory");
    if (lane == 0) lds_add(cn);
    lds_wait(cn, 8);
    STAMP(1);
    f16v o;
#pragma unroll 1
    for (int att = 0; att < 2; att++) {
      f16v ci, sA, sB;
#pragma unroll
      for (int i = 0; i < 16; i++) { ci[i] = -m; o[i] = 0.f; }
      const char* kp = sm + lane * 16;
      const char* vp = sm + v0;
      sA = mfma(*(const u4*)kp, qf, ci);
      u4 k1f = *(const u4*)(kp + 1024);
#pragma unroll 1
      for (int t = 0; t < 64; t += 2) {
        if (t == 14 && att == 0) { lds_arrive(cn + 1, lane); lds_wait(cn + 1, 8); }
        u4 va = *(const u4*)vp, vb = *(const u4*)(vp + 512), vc = *(const u4*)(vp + 1024), vd = *(const u4*)(vp + 1536);
        u4 k2f = *(const u4*)(kp + 2048);
        sB = mfma(k1f, qf, ci);
        k1f = *(const u4*)(kp + 3072);
        u4 p0, p1;
#pragma unroll
        for (int i = 0; i < 4; i++) p0[i] = pk(__builtin_amdgcn_exp2f(sA[2 * i]), __builtin_amdgcn_exp2f(sA[2 * i + 1]));
        o = mfma(va, p0, o);
#pragma unroll
        for (int i = 0; i < 4; i++) p1[i] = pk(__builtin_amdgcn_exp2f(sA[8 + 2 * i]), __builtin_amdgcn_exp2f(sA[9 + 2 * i]));
        o = mfma(vb, p1, o);
        sA = mfma(k2f, qf, ci);
#pragma unroll
        for (int i = 0; i < 4; i++) p0[i] = pk(__builtin_amdgcn_exp2f(sB[2 * i]), __builtin_amdgcn_exp2f(sB[2 * i + 1]));
        o = mfma(vc, p0, o);
#pragma unroll
        for (int i = 0; i < 4; i++) p1[i] = pk(__builtin_amdgcn_exp2f(sB[8 + 2 * i]), __builtin_amdgcn_exp2f(sB[9 + 2 * i]));
        o = mfma(vd, p1, o);
        kp += 2048; vp += vst;
      }
      if (att == 1 || __all(o[8] >= 8.67e-19f && o[8] < 1e30f)) break;
      f16v z;
#pragma unroll
      for (int i = 0; i < 16; i++) z[i] = 0.f;
      float mx = -3.0e38f;
#pragma unroll 1
      for (int t = 0; t < 64; t++) {
        f16v s = mfma(*(const u4*)(sm + lane * 16 + t * 1024), qf, z);
#pragma unroll
        for (int i = 0; i < 16; i++) mx = fmaxf(mx, s[i]);
      }
      m = fmaxf(mx, xor32(mx));
    }
    STAMP(2);
    const float il = 1.0f / o[8];
    u4 r;
#pragma unroll
    for (int i = 0; i < 4; i++) r[i] = pk(o[2 * i] * il, o[2 * i + 1] * il);
    ((u4*)(ws + O_OP))[(qt * 32 + grp) * 64 + lane] = r;
  } else {
    __builtin_amdgcn_s_setprio(3);
    const int gw = wid - 8, mi = gw >> 1, ni = gw & 1;
    GAS unsigned* gc = (GAS unsigned*)(ws + O_CN) + grp * 64;
    const __amdgpu_buffer_rsrc_t rs = __builtin_amdgcn_make_buffer_rsrc((void*)ws, (short)0, 0x7fffffff, 0x00020000);
    const int ft = sub * 2 + mi, tt = grp * 2 + ni;
    {
      f16v acc[1][1];
      u2 xr[4];
#pragma unroll
      for (int q = 0; q < 4; q++) xr[q] = *(const u2*)(x + (tt * 32 + l31) * 512 + ft * 32 + 8 * q + 4 * h);
#pragma unroll
      for (int i = 0; i < 16; i++) acc[0][0][i] = 0.f;
      wgemm<1, 1, 8, 0>(acc, rs, O_W2 + ft * 32768 + lane * 16, 0, O_HS + tt * 32768 + lane * 16, 0, 32);
      float s1 = 0.f, s2 = 0.f;
#pragma unroll
      for (int s = 0; s < 2; s++) {
        u4 v;
#pragma unroll
        for (int i = 0; i < 4; i++) {
          const unsigned xv = xr[2 * s + (i >> 1)][i & 1];
          v[i] = pk(acc[0][0][8 * s + 2 * i] + blo(xv), acc[0][0][8 * s + 2 * i + 1] + bhi(xv));
          s1 = dot2(v[i], ONES, s1); s2 = dot2(v[i], v[i], s2);
        }
        __builtin_amdgcn_raw_buffer_store_b128(v, rs, O_X1 + ((tt * 32 + ft * 2 + s) * 64 + lane) * 16, 0, 16);
      }
      u2 st = {__builtin_bit_cast(unsigned, s1), __builtin_bit_cast(unsigned, s2)};
      __builtin_amdgcn_raw_buffer_store_b64(st, rs, O_ST + ((tt * 16 + ft) * 64 + lane) * 8, 0, 16);
      STAMP(1);
      publish(cn + 2, gc, lane);
      STAMP(2);
    }
    const int j = sub * 4 + gw;
    if (j < 28) {
      f16v acc[2][2];
#pragma unroll
      for (int i = 0; i < 16; i++) { acc[0][0][i] = 0.f; acc[0][1][i] = 0.f; acc[1][0][i] = 0.f; acc[1][1][i] = 0.f; }
      consume(gc, 8);
      STAMP(3);
      float mean[2], rstd[2];
#pragma unroll
      for (int n = 0; n < 2; n++) {
        f2 t = {0.f, 0.f};
#pragma unroll
        for (int f = 0; f < 16; f++) t += __builtin_bit_cast(f2, __builtin_amdgcn_raw_buffer_load_b64(rs, O_ST + (((grp * 2 + n) * 16 + f) * 64 + lane) * 8, 0, 16));
        t[0] += xor32(t[0]); t[1] += xor32(t[1]);
        mean[n] = t[0] * (1.f / 512.f);
        rstd[n] = __builtin_amdgcn_rsqf(fmaxf(t[1] * (1.f / 512.f) - mean[n] * mean[n], 0.f) + 1e-5f);
      }
      wgemm<2, 2, 4, 16>(acc, rs, O_W3 + j * 65536 + lane * 16, 32768, O_X1 + grp * 65536 + lane * 16, 32768, 32);
#pragma unroll
      for (int mm = 0; mm < 2; mm++)
#pragma unroll
        for (int s = 0; s < 2; s++) {
          const float* cp = (const float*)(ws + O_CS) + j * 64 + mm * 32 + 16 * s + 4 * h;
          const f4 c0 = *(const f4*)cp, c1 = *(const f4*)(cp + 8);
#pragma unroll
          for (int n = 0; n < 2; n++) {
            float r[8];
#pragma unroll
            for (int i = 0; i < 4; i++) {
              r[i] = gelu(rstd[n] * (acc[mm][n][8 * s + i] - mean[n] * c0[i]));
              r[4 + i] = gelu(rstd[n] * (acc[mm][n][8 * s + 4 + i] - mean[n] * c1[i]));
            }
            u4 v = {pk(r[0], r[1]), pk(r[2], r[3]), pk(r[4], r[5]), pk(r[6], r[7])};
            __builtin_amdgcn_raw_buffer_store_b128(v, rs, O_H2 + (((grp * 2 + n) * 112 + j * 4 + mm * 2 + s) * 64 + lane) * 16, 0, 16);
          }
        }
      STAMP(4);
      publish(cn + 3, gc + 32, lane);
    }
    {
      f16v acc[2][2];
#pragma unroll
      for (int i = 0; i < 16; i++) { acc[0][0][i] = 0.f; acc[0][1][i] = 0.f; acc[1][0][i] = 0.f; acc[1][1][i] = 0.f; }
      consume(gc + 32, 7);
      STAMP(5);
      const u4 x0 = BLD(O_X1 + ((tt * 32 + ft * 2) * 64 + lane) * 16, 0, 16), x1 = BLD(O_X1 + ((tt * 32 + ft * 2 + 1) * 64 + lane) * 16, 0, 16);
      wgemm<2, 2, 4, 16>(acc, rs, O_W4 + (ft * 112 + gw * 28) * 1024 + lane * 16, (1 - 2 * mi) * 114688,
                         O_H2 + (tt * 112 + gw * 28) * 1024 + lane * 16, (1 - 2 * ni) * 114688, 28);
      STAMP(6);
#pragma unroll
      for (int r = 1; r < 4; r++) {
        f4* dp = (f4*)(sm + L_R + (r ^ gw) * 4096) + lane;
        const f16v a = acc[r >> 1][r & 1];
#pragma unroll
        for (int i = 0; i < 4; i++) dp[i * 64] = (f4){a[4 * i], a[4 * i + 1], a[4 * i + 2], a[4 * i + 3]};
        lds_arrive(cn + 4, lane); lds_wait(cn + 4, 8 * r - 4);
        const f4* sp = (const f4*)(sm + L_R + gw * 4096) + lane;
#pragma unroll
        for (int i = 0; i < 4; i++) {
          const f4 v = sp[i * 64];
          acc[0][0][4 * i] += v[0]; acc[0][0][4 * i + 1] += v[1]; acc[0][0][4 * i + 2] += v[2]; acc[0][0][4 * i + 3] += v[3];
        }
        if (r < 3) { lds_arrive(cn + 4, lane); lds_wait(cn + 4, 8 * r); }
      }
      u4 v, w;
#pragma unroll
      for (int i = 0; i < 4; i++) {
        v[i] = pk(acc[0][0][2 * i] + blo(x0[i]), acc[0][0][2 * i + 1] + bhi(x0[i]));
        w[i] = pk(acc[0][0][8 + 2 * i] + blo(x1[i]), acc[0][0][9 + 2 * i] + bhi(x1[i]));
      }
      ((u4*)(ws + O_BP))[(tt * 32 + ft * 2) * 64 + lane] = v;
      ((u4*)(ws + O_BP))[(tt * 32 + ft * 2 + 1) * 64 + lane] = w;
      STAMP(7);
    }
  }
}

__global__ __launch_bounds__(256, 1) void k3(const char* ws, u16* out) {
  __shared__ __attribute__((aligned(16))) char sm[131072];
  const int b = blockIdx.x, tid = threadIdx.x, lane = tid & 63, wid = __builtin_amdgcn_readfirstlane(tid >> 6);
  const int h = lane >> 5, l31 = lane & 31;
  const int grp = (b & 7) * 4 + ((b >> 3) & 3), sub = b >> 5;
  const int mi = wid >> 1, ni = wid & 1, ft = sub * 2 + mi, tt = grp * 2 + ni;
  const char* src = ws + (wid < 2 ? O_WO + (sub * 2 + wid) * 32768 : O_OP + (grp * 2 + wid - 2) * 32768);
#pragma unroll
  for (int k = 0; k < 32; k++) glds(src + k * 1024, lane * 16, laddr(sm) + wid * 32768 + k * 1024);
  f16v acc;
#pragma unroll
  for (int i = 0; i < 16; i++) acc[i] = 0.f;
  const char* ap = sm + mi * 32768 + lane * 16;
  const char* bp = sm + 65536 + ni * 32768 + lane * 16;
  u4 b0, b1; float g;
#pragma unroll
  for (int p = 0; p < 4; p++) {
    if (p == 0) WAITV(24); else if (p == 1) WAITV(16); else if (p == 2) WAITV(8); else WAITV(0);
    BAR();
    if (p == 3) {
      b0 = ((const u4*)(ws + O_BP))[(tt * 32 + ft * 2) * 64 + lane];
      b1 = ((const u4*)(ws + O_BP))[(tt * 32 + ft * 2 + 1) * 64 + lane];
      g = ((const float*)(ws + O_G))[tt * 32 + l31];
    }
#pragma unroll
    for (int k = 0; k < 8; k++) acc = mfma(*(const u4*)(ap + (p * 8 + k) * 1024), *(const u4*)(bp + (p * 8 + k) * 1024), acc);
  }
  const float g1 = 1.0f - g;
#pragma unroll
  for (int s = 0; s < 2; s++) {
    const u4 bb = s ? b1 : b0;
    unsigned r[4];
#pragma unroll
    for (int i = 0; i < 4; i++) r[i] = pk(g * acc[8 * s + 2 * i] + g1 * blo(bb[i]), g * acc[8 * s + 2 * i + 1] + g1 * bhi(bb[i]));
    auto e0 = __builtin_amdgcn_permlane32_swap(r[0], r[2], false, false);
    auto e1 = __builtin_amdgcn_permlane32_swap(r[1], r[3], false, false);
    u4 v = {e0[0], e1[0], e0[1], e1[1]};
    *(u4*)(out + (tt * 32 + l31) * 512 + ft * 32 + 16 * s + 8 * h) = v;
  }
}

extern "C" void kernel_launch(void* const* d_in, const int* in_sizes, int n_in, void* d_out, int out_size, void* d_ws,
                              size_t ws_size, hipStream_t stream) {
  const u16* x = (const u16*)d_in[0];
  char* ws = (char*)d_ws;
  k1<<<256 + 288 + 28, 256, 0, stream>>>(x, (const u16*)d_in[1], (const u16*)d_in[2], (const u16*)d_in[4], (const u16*)d_in[5],
                                         (const u16*)d_in[6], (const u16*)d_in[7], (const u16*)d_in[8], ws);
  k2<<<256, 768, 0, stream>>>(x, ws);
  k3<<<256, 256, 0, stream>>>(ws, (u16*)d_out);
}
```

```cpp
#include <hip/hip_runtime.h>
#include <hip/hip_bf16.h>

typedef unsigned short u16;
typedef __bf16 bf8 __attribute__((ext_vector_type(8)));
typedef __bf16 bf2 __attribute__((ext_vector_type(2)));
typedef float f16v __attribute__((ext_vector_type(16)));
typedef float f4 __attribute__((ext_vector_type(4)));
typedef float f2 __attribute__((ext_vector_type(2)));
typedef unsigned u4 __attribute__((ext_vector_type(4)));
typedef unsigned u2 __attribute__((ext_vector_type(2)));
typedef short s4 __attribute__((ext_vector_type(4)));
#define LAS __attribute__((address_space(3)))
#define GAS __attribute__((address_space(1)))
#define DI __device__ __forceinline__

#define MB (1u << 20)
#define O_QP (0u)
#define O_KP (2u * MB)
#define O_VP (4u * MB)
#define O_HS (6u * MB)
#define O_X1 (8u * MB)
#define O_BP (10u * MB)
#define O_OP (12u * MB)
#define O_H2 (14u * MB)
#define O_W2 (21u * MB)
#define O_WO (21u * MB + 512u * 1024u)
#define O_W3 (22u * MB)
#define O_W4 (24u * MB)
#define O_ST (26u * MB)
#define O_CS (27u * MB)
#define O_G (27u * MB + 8192u)
#define O_KM (27u * MB + 16384u)
#define O_CN (27u * MB + 32768u)

DI f16v mfma(u4 a, u4 b, f16v c) {
  return __builtin_amdgcn_mfma_f32_32x32x16_bf16(__builtin_bit_cast(bf8, a), __builtin_bit_cast(bf8, b), c, 0, 0, 0);
}
DI unsigned pk(float a, float b) {
  f2 v = {a, b};
  return __builtin_bit_cast(unsigned, __builtin_convertvector(v, bf2));
}
DI float dot2(unsigned a, unsigned b, float c) {
  return __builtin_amdgcn_fdot2_f32_bf16(__builtin_bit_cast(bf2, a), __builtin_bit_cast(bf2, b), c, false);
}
DI float blo(unsigned v) { return __builtin_bit_cast(float, v << 16); }
DI float bhi(unsigned v) { return __builtin_bit_cast(float, v & 0xffff0000u); }
DI float gelu(float v) {
  float e = __builtin_amdgcn_exp2f(v * __builtin_fmaf(v * v, -0.1029432397f, -2.3022081985f));
  return v * __builtin_amdgcn_rcpf(1.0f + e);
}
DI float xor32(float v) { return __shfl_xor(v, 32); }
DI void glds(const void* base, unsigned voff, unsigned l) {
  unsigned keep;
  asm volatile("s_mov_b32 %0, m0\n\ts_mov_b32 m0, %3\n\ts_nop 0\n\tglobal_load_lds_dwordx4 %1, %2\n\ts_mov_b32 m0, %0"
               : "=&s"(keep) : "v"(voff), "s"(base), "s"(l) : "memory");
}
DI unsigned laddr(char* p) { return __builtin_amdgcn_readfirstlane((unsigned)(size_t)(LAS char*)p); }
#define WAITV(n) asm volatile("s_waitcnt vmcnt(" #n ")" ::: "memory")
#define BAR() do { asm volatile("s_waitcnt lgkmcnt(0)" ::: "memory"); __builtin_amdgcn_s_barrier(); asm volatile("" ::: "memory"); } while (0)
#define ONES 0x3f803f80u

#define STAMP1(k)
#define STAMP(k)
template <int MODE>
DI void k1_gemm(char* sm, const u16* X, const u16* W, int ldw, int nb, int tb, const u16* gate, char* ws) {
  const int tid = threadIdx.x, lane = tid & 63, wid = __builtin_amdgcn_readfirstlane(tid >> 6);
  const int wr = wid >> 1, wc = wid & 1, h = lane >> 5, l31 = lane & 31;
  const unsigned xs = ((32 * wid + (lane >> 2)) * 512 + (((lane & 3) ^ ((lane >> 4) & 3)) * 8)) * 2;
  const unsigned wsA = ((8 * wid + (lane >> 4)) * ldw + (((lane & 15) ^ (((lane >> 4) << 2) | ((2 * wid) & 3))) * 8)) * 2;
  const unsigned wsB = ((8 * wid + 4 + (lane >> 4)) * ldw + (((lane & 15) ^ (((lane >> 4) << 2) | ((2 * wid + 1) & 3))) * 8)) * 2;
  const unsigned xl = laddr(sm) + wid * 2048, wl = xl + 8192;
  if (MODE == 0 && wid == 0) glds(gate, lane * 16, laddr(sm) + 65536);
  const int rot = ((MODE + tb) & 3) * 4;
#define STAGE(kc) do { const int kk = ((kc) + rot) & 15; \
    glds(X + kk * 32, xs, xl + ((kc) & 3) * 16384); glds(X + 16 * 512 + kk * 32, xs, xl + ((kc) & 3) * 16384 + 1024); \
    glds(W + kk * 32 * ldw, wsA, wl + ((kc) & 3) * 16384); glds(W + kk * 32 * ldw, wsB, wl + ((kc) & 3) * 16384 + 1024); } while (0)
  STAGE(0); STAGE(1); STAGE(2); STAGE(3);
  const int g1 = (lane >> 4) & 1, q = (lane & 15) >> 2, p = lane & 3;
  int aoff[2][2], boff[2];
  const int pn = MODE == 3 ? wc : 0, pt = MODE == 3 ? wr : 0;
#pragma unroll
  for (int nt = 0; nt < 2; nt++)
#pragma unroll
    for (int e = 0; e < 2; e++)
      aoff[nt][e] = 8192 + 256 * (8 * h + 4 * e + q) + 16 * ((8 * wr + 4 * (nt ^ pn) + 2 * g1 + (p >> 1)) ^ ((q << 2) | (2 * h + e))) + 8 * (p & 1);
#pragma unroll
  for (int s = 0; s < 2; s++) boff[s] = (64 * wc + l31) * 64 + (((2 * s + h) ^ ((l31 >> 2) & 3)) * 16);
  f16v acc[2][2];
#pragma unroll
  for (int i = 0; i < 16; i++) { acc[0][0][i] = 0.f; acc[0][1][i] = 0.f; acc[1][0][i] = 0.f; acc[1][1][i] = 0.f; }
  float s1[2] = {0.f, 0.f}, s2[2] = {0.f, 0.f}, cs[2] = {0.f, 0.f};
  u4 fa[2][2], fb[2][2], fg[2];
  auto rd = [&](int kc, int s) {
    const char* sb = sm + (kc & 3) * 16384;
#pragma unroll
    for (int nt = 0; nt < 2; nt++) {
      s4 lo = __builtin_amdgcn_ds_read_tr16_b64_v4i16((LAS s4*)(sb + aoff[nt][0] + s * 4096));
      s4 hi = __builtin_amdgcn_ds_read_tr16_b64_v4i16((LAS s4*)(sb + aoff[nt][1] + s * 4096));
      u2 l2 = __builtin_bit_cast(u2, lo), h2 = __builtin_bit_cast(u2, hi);
      fa[s][nt] = (u4){l2[0], l2[1], h2[0], h2[1]};
      fb[s][nt] = *(const u4*)(sb + boff[s] + (nt ^ pt) * 2048);
    }
    if (MODE == 0) fg[s] = *(const u4*)(sm + 65536 + (((kc + rot) & 15) * 32 + s * 16 + h * 8) * 2);
  };
  auto mm = [&](int s) {
#pragma unroll
    for (int nt = 0; nt < 2; nt++)
#pragma unroll
      for (int tt = 0; tt < 2; tt++)
        acc[nt][tt] = (MODE == 2) ? mfma(fb[s][tt], fa[s][nt], acc[nt][tt]) : mfma(fa[s][nt], fb[s][tt], acc[nt][tt]);
    if (MODE == 3) {
#pragma unroll
      for (int i = 0; i < 4; i++) {
        s1[0] = dot2(fb[s][0][i], ONES, s1[0]); s2[0] = dot2(fb[s][0][i], fb[s][0][i], s2[0]); cs[0] = dot2(fa[s][0][i], ONES, cs[0]);
      }
#pragma unroll
      for (int i = 0; i < 4; i++) { __builtin_amdgcn_sched_group_barrier(8, 1, 0); __builtin_amdgcn_sched_group_barrier(2, 3, 0); }
    }
    if (MODE == 0) {
      if (s == wr) {
#pragma unroll
        for (int tt = 0; tt < 2; tt++)
#pragma unroll
          for (int i = 0; i < 4; i++) s1[tt] = dot2(fb[s][tt][i], fg[s][i], s1[tt]);
      }
    }
  };
  WAITV(12);
  BAR();
  STAMP1(1);
  rd(0, 0);
#pragma unroll
  for (int kc = 0; kc < 16; kc++) {
    rd(kc, 1);
    __builtin_amdgcn_sched_barrier(0);
    mm(0);
    __builtin_amdgcn_sched_barrier(0);
    if (kc < 15) {
      if (kc < 13) WAITV(8); else if (kc == 13) WAITV(4); else WAITV(0);
      BAR();
      rd(kc + 1, 0);
      if (kc + 4 < 16) STAGE(kc + 4);
    }
    __builtin_amdgcn_sched_barrier(0);
    mm(1);
    __builtin_amdgcn_sched_barrier(0);
  }
#undef STAGE
  STAMP1(2);
  const int T0 = tb * 4 + wc * 2;
  if (MODE == 0 || MODE == 1) {
    u4* dst = (u4*)(ws + (MODE == 0 ? O_QP : O_KP));
    const float sc = MODE == 0 ? 0.25f * 1.4426950409f : 1.0f;
#pragma unroll
    for (int nt = 0; nt < 2; nt++)
#pragma unroll
      for (int hh = 0; hh < 2; hh++) {
        const int head = (nb & 3) * 8 + wr * 4 + nt * 2 + hh;
        float mx = 0.f;
#pragma unroll
        for (int tt = 0; tt < 2; tt++) {
          u4 v;
#pragma unroll
          for (int i = 0; i < 4; i++) v[i] = pk(acc[nt][tt][8 * hh + 2 * i] * sc, acc[nt][tt][8 * hh + 2 * i + 1] * sc);
          dst[(head * 64 + T0 + tt) * 64 + lane] = v;
          if (MODE == 1) {
            float n2 = dot2(v[0], v[0], dot2(v[1], v[1], dot2(v[2], v[2], dot2(v[3], v[3], 0.f))));
            mx = fmaxf(mx, n2 + xor32(n2));
          }
        }
        if (MODE == 1) {
#pragma unroll
          for (int o = 16; o >= 1; o >>= 1) mx = fmaxf(mx, __shfl_xor(mx, o));
          if (lane == 0) ((float*)(ws + O_KM))[head * 32 + tb * 2 + wc] = mx;
        }
      }
    if (MODE == 0 && nb == 0) {
      float* red = (float*)sm;
      BAR();
      s1[0] += xor32(s1[0]); s1[1] += xor32(s1[1]);
      red[wr * 128 + wc * 64 + lane] = h ? s1[1] : s1[0];
      BAR();
      if (tid < 128) {
        float z = red[tid] + red[128 + tid];
        ((float*)(ws + O_G))[tb * 128 + tid] = __builtin_amdgcn_rcpf(1.0f + __builtin_amdgcn_exp2f(-1.4426950409f * z));
      }
    }
  } else if (MODE == 2) {
    u4* dst = (u4*)(ws + O_VP);
#pragma unroll
    for (int nt = 0; nt < 2; nt++) {
      const int head = (nb & 3) * 8 + wr * 4 + nt * 2 + g1;
#pragma unroll
      for (int tt = 0; tt < 2; tt++)
#pragma unroll
        for (int s = 0; s < 2; s++) {
          u4 v;
#pragma unroll
          for (int i = 0; i < 4; i++) v[i] = pk(acc[nt][tt][8 * s + 2 * i], acc[nt][tt][8 * s + 2 * i + 1]);
          dst[(head * 64 + T0 + tt) * 64 + s * 32 + h * 16 + (lane & 15)] = v;
        }
    }
  } else {
    float* red = (float*)sm;
    BAR();
    red[wc * 64 + wr * 32 + l31] = s1[0] + xor32(s1[0]);
    red[128 + wc * 64 + wr * 32 + l31] = s2[0] + xor32(s2[0]);
    red[256 + wr * 64 + wc * 32 + l31] = cs[0] + xor32(cs[0]);
    BAR();
    const int grp = nb & 3;
    const int dl = grp == 0 ? 1 : grp == 1 ? -1 : grp == 2 ? 2 : -2;
    u4* dst = (u4*)(ws + O_HS);
#pragma unroll
    for (int tt = 0; tt < 2; tt++) {
      const int tl = wc * 64 + (tt ^ wr) * 32 + l31;
      const float mean = red[tl] * (1.f / 512.f);
      const float var = fmaxf(red[128 + tl] * (1.f / 512.f) - mean * mean, 0.f);
      const float rstd = __builtin_amdgcn_rsqf(var + 1e-5f);
      const int td = tb * 128 + tl + dl;
      const bool ok = (unsigned)td < 2048u;
      const int tw = td & 2047;
#pragma unroll
      for (int nt = 0; nt < 2; nt++)
#pragma unroll
        for (int s = 0; s < 2; s++) {
          u4 v;
#pragma unroll
          for (int jj = 0; jj < 2; jj++) {
            const int f = wr * 64 + (nt ^ wc) * 32 + 16 * s + 8 * jj + 4 * h;
            f4 c = *(const f4*)(red + 256 + f);
            float r[4];
#pragma unroll
            for (int i = 0; i < 4; i++) r[i] = gelu(rstd * (acc[nt][tt][8 * s + 4 * jj + i] - mean * c[i]));
            v[2 * jj] = ok ? pk(r[0], r[1]) : 0u;
            v[2 * jj + 1] = ok ? pk(r[2], r[3]) : 0u;
          }
          dst[((tw >> 5) * 32 + grp * 8 + wr * 4 + (nt ^ wc) * 2 + s) * 64 + h * 32 + (tw & 31)] = v;
        }
    }
  }
}

__global__ __launch_bounds__(256, 2) void k1(const u16* x, const u16* wqkv, const u16* wo, const u16* w1, const u16* w2,
                                             const u16* w3, const u16* w4, const u16* gate, char* ws) {
  __shared__ __attribute__((aligned(16))) char sm[65536 + 1024];
  const int b = blockIdx.x, tid = threadIdx.x;
  STAMP1(0);
  if (b < 256) {
    const int xcd = b & 7, i = b >> 3;
    const int md = i & 3, nn = md * 4 + (xcd & 3), tb = (xcd >> 2) * 8 + (i >> 2);
    const u16* X = x + tb * 128 * 512;
    if (md == 0) k1_gemm<0>(sm, X, wqkv + nn * 128, 1536, nn, tb, gate, ws);
    else if (md == 1) k1_gemm<1>(sm, X, wqkv + nn * 128, 1536, nn, tb, gate, ws);
    else if (md == 2) k1_gemm<2>(sm, X, wqkv + nn * 128, 1536, nn, tb, gate, ws);
    else k1_gemm<3>(sm, X, w1 + (nn - 12) * 128, 512, nn, tb, gate, ws);
  } else if (b >= 284) {
    int u = (b - 284) * 256 + tid;
    if (b == 284 && tid < 64) ((unsigned*)(ws + O_CN))[tid * 32] = 0u;
    const u16* src; u4* dst; int N, KS;
    if (u < 8192) { src = w2; dst = (u4*)(ws + O_W2); N = 512; KS = 32; }
    else if (u < 16384) { u -= 8192; src = wo; dst = (u4*)(ws + O_WO); N = 512; KS = 32; }
    else if (u < 45056) { u -= 16384; src = w3; dst = (u4*)(ws + O_W3); N = 1792; KS = 32; }
    else { u -= 45056; src = w4; dst = (u4*)(ws + O_W4); N = 512; KS = 112; }
    const int nq = N >> 2, r = u / nq, n0 = (u - r * nq) * 4, h = r & 1, ks = r >> 1;
    u2 v[8];
#pragma unroll
    for (int j = 0; j < 8; j++) v[j] = *(const u2*)(src + (16 * ks + 8 * (j >> 2) + 4 * h + (j & 3)) * N + n0);
    u4* d = dst + ((n0 >> 5) * KS + ks) * 64 + h * 32 + (n0 & 31);
#pragma unroll
    for (int c = 0; c < 4; c++) {
      u4 o;
#pragma unroll
      for (int dd = 0; dd < 4; dd++)
        o[dd] = __builtin_amdgcn_perm(v[2 * dd + 1][c >> 1], v[2 * dd][c >> 1], (c & 1) ? 0x07060302u : 0x05040100u);
      d[c] = o;
    }
  } else {
    const int cb = b - 256, cq = tid & 15, sl = tid >> 4;
    const u16* src = w3 + (sl * 32) * 1792 + cb * 64 + cq * 4;
    f4 a = {0.f, 0.f, 0.f, 0.f};
#pragma unroll
    for (int r = 0; r < 32; r++) {
      u2 v = *(const u2*)(src + r * 1792);
      a[0] += blo(v[0]); a[1] += bhi(v[0]); a[2] += blo(v[1]); a[3] += bhi(v[1]);
    }
    *(f4*)(sm + (sl * 64 + cq * 4) * 4) = a;
    __syncthreads();
    if (tid < 64) {
      float t = 0.f;
#pragma unroll
      for (int r = 0; r < 16; r++) t += ((const float*)sm)[r * 64 + tid];
      ((float*)(ws + O_CS))[cb * 64 + tid] = t;
    }
  }
  STAMP1(3);
}

typedef __amdgpu_buffer_rsrc_t rsrc_t;
#define BLD(off, so, aux) __builtin_amdgcn_raw_buffer_load_b128(rs, (off), (so), (aux))
#define SB() __builtin_amdgcn_sched_barrier(0)
template <int MT, int NT, int D, int AUX>
DI void wgemm(f16v (&acc)[MT][NT], rsrc_t rs, int A, int sa, int B, int sb, int nks) {
  u4 a[D][MT], b[D][NT];
#pragma unroll
  for (int d = 0; d < D; d++) {
#pragma unroll
    for (int m = 0; m < MT; m++) a[d][m] = BLD(A + m * sa + (d & 3) * 1024, (d >> 2) * 4096, 0);
#pragma unroll
    for (int n = 0; n < NT; n++) b[d][n] = BLD(B + n * sb + (d & 3) * 1024, (d >> 2) * 4096, AUX);
    SB();
  }
#pragma unroll 1
  for (int kb = D; kb < nks; kb += D) {
#pragma unroll
    for (int d = 0; d < D; d++) {
#pragma unroll
      for (int m = 0; m < MT; m++)
#pragma unroll
        for (int n = 0; n < NT; n++) acc[m][n] = mfma(a[d][m], b[d][n], acc[m][n]);
      SB();
#pragma unroll
      for (int m = 0; m < MT; m++) a[d][m] = BLD(A + m * sa + (d & 3) * 1024, kb * 1024 + (d >> 2) * 4096, 0);
#pragma unroll
      for (int n = 0; n < NT; n++) b[d][n] = BLD(B + n * sb + (d & 3) * 1024, kb * 1024 + (d >> 2) * 4096, AUX);
      SB();
    }
  }
#pragma unroll
  for (int d = 0; d < D; d++)
#pragma unroll
    for (int m = 0; m < MT; m++)
#pragma unroll
      for (int n = 0; n < NT; n++) acc[m][n] = mfma(a[d][m], b[d][n], acc[m][n]);
}
DI unsigned lds_add(unsigned* c) { return __hip_atomic_fetch_add(c, 1u, __ATOMIC_RELAXED, __HIP_MEMORY_SCOPE_WORKGROUP); }
DI void lds_arrive(unsigned* c, int lane) {
  asm volatile("s_waitcnt vmcnt(0) lgkmcnt(0)" ::: "memory");
  if (lane == 0) lds_add(c);
}
DI void lds_wait(unsigned* c, unsigned n) {
  while (__hip_atomic_load(c, __ATOMIC_RELAXED, __HIP_MEMORY_SCOPE_WORKGROUP) < n) __builtin_amdgcn_s_sleep(1);
  asm volatile("" ::: "memory");
}
DI void publish(unsigned* lc, GAS unsigned* gc, int lane) {
  asm volatile("s_waitcnt vmcnt(0)" ::: "memory");
  if (lane == 0) {
    if (lds_add(lc) == 3u) __hip_atomic_fetch_add(gc, 1u, __ATOMIC_RELAXED, __HIP_MEMORY_SCOPE_AGENT);
  }
}
DI void consume(GAS unsigned* gc, unsigned n) {
  while (__hip_atomic_load(gc, __ATOMIC_RELAXED, __HIP_MEMORY_SCOPE_AGENT) < n) __builtin_amdgcn_s_sleep(2);
  __builtin_amdgcn_fence(__ATOMIC_ACQUIRE, "wavefront");
  asm volatile("" ::: "memory");
}
#define L_V 65536
#define L_C 131072
#define L_R 133120
#define L_N 149504

__global__ __launch_bounds__(768, 3) void k2(const u16* x, char* ws) {
  __shared__ __attribute__((aligned(16))) char sm[L_N + 64];
  const int b = blockIdx.x, tid = threadIdx.x, lane = tid & 63, wid = __builtin_amdgcn_readfirstlane(tid >> 6);
  const int h = lane >> 5, l31 = lane & 31;
  const int grp = (b & 7) * 4 + ((b >> 3) & 3), sub = b >> 5;
  unsigned* cn = (unsigned*)(sm + L_N);
  const int qt = sub * 8 + wid;
  u4 qf; float km;
  STAMP(0);
  if (wid < 8) {
    qf = ((const u4*)(ws + O_QP))[(grp * 64 + qt) * 64 + lane];
    km = ((const float*)(ws + O_KM))[grp * 32 + l31];
    asm volatile("" ::: "memory");
    const char* kg = ws + O_KP + grp * 65536 + wid * 2048;
    const unsigned lb = laddr(sm) + wid * 2048, vo = lane * 16;
#pragma unroll
    for (int p = 0; p < 4; p++) {
      glds(kg + p * 16384, vo, lb + p * 16384);
      glds(kg + p * 16384 + 1024, vo, lb + p * 16384 + 1024);
      glds(kg + 2 * MB + p * 16384, vo, lb + L_V + p * 16384);
      glds(kg + 2 * MB + p * 16384 + 1024, vo, lb + L_V + p * 16384 + 1024);
    }
  } else {
    const int t = tid - 512;
    if (t < 128) { const unsigned v = (t & 31) == 0 ? ONES : 0u; *(u4*)(sm + L_C + t * 16) = (u4){v, v, v, v}; }
    if (t < 16) cn[t] = 0u;
  }
  BAR();
  if (wid < 8) {
#pragma unroll
    for (int o = 16; o >= 1; o >>= 1) km = fmaxf(km, __shfl_xor(km, o));
    float qn = dot2(qf[0], qf[0], dot2(qf[1], qf[1], dot2(qf[2], qf[2], dot2(qf[3], qf[3], 0.f))));
    qn += xor32(qn);
    float m = __builtin_sqrtf(qn * km) * 1.001f;
    const bool real = l31 < 16;
    const int vst = real ? 2048 : 0;
    const int v0 = real ? L_V + h * 256 + l31 * 16 : L_C + ((l31 & 3) == 0 && l31 < 24 ? 0 : 16);
    asm volatile("s_waitcnt vmcnt(12)" ::: "memory");
    if (lane == 0) lds_add(cn);
    lds_wait(cn, 8);
    STAMP(1);
    f16v o;
#pragma unroll 1
    for (int att = 0; att < 2; att++) {
      f16v ci, sA, sB;
#pragma unroll
      for (int i = 0; i < 16; i++) { ci[i] = -m; o[i] = 0.f; }
      const char* kp = sm + lane * 16;
      const char* vp = sm + v0;
      sA = mfma(*(const u4*)kp, qf, ci);
      u4 k1f = *(const u4*)(kp + 1024);
#pragma unroll 1
      for (int t = 0; t < 64; t += 2) {
        if (t == 14 && att == 0) { lds_arrive(cn + 1, lane); lds_wait(cn + 1, 8); }
        u4 va = *(const u4*)vp, vb = *(const u4*)(vp + 512), vc = *(const u4*)(vp + 1024), vd = *(const u4*)(vp + 1536);
        u4 k2f = *(const u4*)(kp + 2048);
        sB = mfma(k1f, qf, ci);
        k1f = *(const u4*)(kp + 3072);
        u4 p0, p1;
#pragma unroll
        for (int i = 0; i < 4; i++) p0[i] = pk(__builtin_amdgcn_exp2f(sA[2 * i]), __builtin_amdgcn_exp2f(sA[2 * i + 1]));
        o = mfma(va, p0, o);
#pragma unroll
        for (int i = 0; i < 4; i++) p1[i] = pk(__builtin_amdgcn_exp2f(sA[8 + 2 * i]), __builtin_amdgcn_exp2f(sA[9 + 2 * i]));
        o = mfma(vb, p1, o);
        sA = mfma(k2f, qf, ci);
#pragma unroll
        for (int i = 0; i < 4; i++) p0[i] = pk(__builtin_amdgcn_exp2f(sB[2 * i]), __builtin_amdgcn_exp2f(sB[2 * i + 1]));
        o = mfma(vc, p0, o);
#pragma unroll
        for (int i = 0; i < 4; i++) p1[i] = pk(__builtin_amdgcn_exp2f(sB[8 + 2 * i]), __builtin_amdgcn_exp2f(sB[9 + 2 * i]));
        o = mfma(vd, p1, o);
        kp += 2048; vp += vst;
      }
      if (att == 1 || __all(o[8] >= 8.67e-19f && o[8] < 1e30f)) break;
      f16v z;
#pragma unroll
      for (int i = 0; i < 16; i++) z[i] = 0.f;
      float mx = -3.0e38f;
#pragma unroll 1
      for (int t = 0; t < 64; t++) {
        f16v s = mfma(*(const u4*)(sm + lane * 16 + t * 1024), qf, z);
#pragma unroll
        for (int i = 0; i < 16; i++) mx = fmaxf(mx, s[i]);
      }
      m = fmaxf(mx, xor32(mx));
    }
    STAMP(2);
    const float il = 1.0f / o[8];
    u4 r;
#pragma unroll
    for (int i = 0; i < 4; i++) r[i] = pk(o[2 * i] * il, o[2 * i + 1] * il);
    ((u4*)(ws + O_OP))[(qt * 32 + grp) * 64 + lane] = r;
  } else {
#define PRIO 3
    __builtin_amdgcn_s_setprio(PRIO);
    const int gw = wid - 8, mi = gw >> 1, ni = gw & 1;
    GAS unsigned* gc = (GAS unsigned*)(ws + O_CN) + grp * 64;
    const __amdgpu_buffer_rsrc_t rs = __builtin_amdgcn_make_buffer_rsrc((void*)ws, (short)0, 0x7fffffff, 0x00020000);
    const int ft = sub * 2 + mi, tt = grp * 2 + ni;
    {
      f16v acc[1][1];
      u2 xr[4];
#pragma unroll
      for (int q = 0; q < 4; q++) xr[q] = *(const u2*)(x + (tt * 32 + l31) * 512 + ft * 32 + 8 * q + 4 * h);
#pragma unroll
      for (int i = 0; i < 16; i++) acc[0][0][i] = 0.f;
      wgemm<1, 1, 8, 0>(acc, rs, O_W2 + ft * 32768 + lane * 16, 0, O_HS + tt * 32768 + lane * 16, 0, 32);
      float s1 = 0.f, s2 = 0.f;
#pragma unroll
      for (int s = 0; s < 2; s++) {
        u4 v;
#pragma unroll
        for (int i = 0; i < 4; i++) {
          const unsigned xv = xr[2 * s + (i >> 1)][i & 1];
          v[i] = pk(acc[0][0][8 * s + 2 * i] + blo(xv), acc[0][0][8 * s + 2 * i + 1] + bhi(xv));
          s1 = dot2(v[i], ONES, s1); s2 = dot2(v[i], v[i], s2);
        }
        __builtin_amdgcn_raw_buffer_store_b128(v, rs, O_X1 + ((tt * 32 + ft * 2 + s) * 64 + lane) * 16, 0, 16);
      }
      u2 st = {__builtin_bit_cast(unsigned, s1), __builtin_bit_cast(unsigned, s2)};
      __builtin_amdgcn_raw_buffer_store_b64(st, rs, O_ST + ((tt * 16 + ft) * 64 + lane) * 8, 0, 16);
      STAMP(1);
      publish(cn + 2, gc, lane);
      STAMP(2);
    }
    const int j = sub * 4 + gw;
    if (j < 28) {
      f16v acc[2][2];
#pragma unroll
      for (int i = 0; i < 16; i++) { acc[0][0][i] = 0.f; acc[0][1][i] = 0.f; acc[1][0][i] = 0.f; acc[1][1][i] = 0.f; }
      consume(gc, 8);
      STAMP(3);
      float mean[2], rstd[2];
#pragma unroll
      for (int n = 0; n < 2; n++) {
        f2 t = {0.f, 0.f};
#pragma unroll
        for (int f = 0; f < 16; f++) t += __builtin_bit_cast(f2, __builtin_amdgcn_raw_buffer_load_b64(rs, O_ST + (((grp * 2 + n) * 16 + f) * 64 + lane) * 8, 0, 16));
        t[0] += xor32(t[0]); t[1] += xor32(t[1]);
        mean[n] = t[0] * (1.f / 512.f);
        rstd[n] = __builtin_amdgcn_rsqf(fmaxf(t[1] * (1.f / 512.f) - mean[n] * mean[n], 0.f) + 1e-5f);
      }
      wgemm<2, 2, 4, 16>(acc, rs, O_W3 + j * 65536 + lane * 16, 32768, O_X1 + grp * 65536 + lane * 16, 32768, 32);
#pragma unroll
      for (int mm = 0; mm < 2; mm++)
#pragma unroll
        for (int s = 0; s < 2; s++) {
          const float* cp = (const float*)(ws + O_CS) + j * 64 + mm * 32 + 16 * s + 4 * h;
          const f4 c0 = *(const f4*)cp, c1 = *(const f4*)(cp + 8);
#pragma unroll
          for (int n = 0; n < 2; n++) {
            float r[8];
#pragma unroll
            for (int i = 0; i < 4; i++) {
              r[i] = gelu(rstd[n] * (acc[mm][n][8 * s + i] - mean[n] * c0[i]));
              r[4 + i] = gelu(rstd[n] * (acc[mm][n][8 * s + 4 + i] - mean[n] * c1[i]));
            }
            u4 v = {pk(r[0], r[1]), pk(r[2], r[3]), pk(r[4], r[5]), pk(r[6], r[7])};
            __builtin_amdgcn_raw_buffer_store_b128(v, rs, O_H2 + (((grp * 2 + n) * 112 + j * 4 + mm * 2 + s) * 64 + lane) * 16, 0, 16);
          }
        }
      STAMP(4);
      publish(cn + 3, gc + 32, lane);
    }
    {
      f16v acc[2][2];
#pragma unroll
      for (int i = 0; i < 16; i++) { acc[0][0][i] = 0.f; acc[0][1][i] = 0.f; acc[1][0][i] = 0.f; acc[1][1][i] = 0.f; }
      consume(gc + 32, 7);
      STAMP(5);
      const u4 x0 = BLD(O_X1 + ((tt * 32 + ft * 2) * 64 + lane) * 16, 0, 16), x1 = BLD(O_X1 + ((tt * 32 + ft * 2 + 1) * 64 + lane) * 16, 0, 16);
      wgemm<2, 2, 4, 16>(acc, rs, O_W4 + (ft * 112 + gw * 28) * 1024 + lane * 16, (1 - 2 * mi) * 114688,
                         O_H2 + (tt * 112 + gw * 28) * 1024 + lane * 16, (1 - 2 * ni) * 114688, 28);
      STAMP(6);
#pragma unroll
      for (int r = 1; r < 4; r++) {
        f4* dp = (f4*)(sm + L_R + (r ^ gw) * 4096) + lane;
        const f16v a = acc[r >> 1][r & 1];
#pragma unroll
        for (int i = 0; i < 4; i++) dp[i * 64] = (f4){a[4 * i], a[4 * i + 1], a[4 * i + 2], a[4 * i + 3]};
        lds_arrive(cn + 4, lane); lds_wait(cn + 4, 8 * r - 4);
        const f4* sp = (const f4*)(sm + L_R + gw * 4096) + lane;
#pragma unroll
        for (int i = 0; i < 4; i++) {
          const f4 v = sp[i * 64];
          acc[0][0][4 * i] += v[0]; acc[0][0][4 * i + 1] += v[1]; acc[0][0][4 * i + 2] += v[2]; acc[0][0][4 * i + 3] += v[3];
        }
        if (r < 3) { lds_arrive(cn + 4, lane); lds_wait(cn + 4, 8 * r); }
      }
      u4 v, w;
#pragma unroll
      for (int i = 0; i < 4; i++) {
        v[i] = pk(acc[0][0][2 * i] + blo(x0[i]), acc[0][0][2 * i + 1] + bhi(x0[i]));
        w[i] = pk(acc[0][0][8 + 2 * i] + blo(x1[i]), acc[0][0][9 + 2 * i] + bhi(x1[i]));
      }
      ((u4*)(ws + O_BP))[(tt * 32 + ft * 2) * 64 + lane] = v;
      ((u4*)(ws + O_BP))[(tt * 32 + ft * 2 + 1) * 64 + lane] = w;
      STAMP(7);
    }
  }
}

__global__ __launch_bounds__(256, 1) void k3(const char* ws, u16* out) {
  __shared__ __attribute__((aligned(16))) char sm[131072];
  const int b = blockIdx.x, tid = threadIdx.x, lane = tid & 63, wid = __builtin_amdgcn_readfirstlane(tid >> 6);
  const int h = lane >> 5, l31 = lane & 31;
  const int grp = (b & 7) * 4 + ((b >> 3) & 3), sub = b >> 5;
  const int mi = wid >> 1, ni = wid & 1, ft = sub * 2 + mi, tt = grp * 2 + ni;
  const char* src = ws + (wid < 2 ? O_WO + (sub * 2 + wid) * 32768 : O_OP + (grp * 2 + wid - 2) * 32768);
#pragma unroll
  for (int k = 0; k < 32; k++) glds(src + k * 1024, lane * 16, laddr(sm) + wid * 32768 + k * 1024);
  f16v acc;
#pragma unroll
  for (int i = 0; i < 16; i++) acc[i] = 0.f;
  const char* ap = sm + mi * 32768 + lane * 16;
  const char* bp = sm + 65536 + ni * 32768 + lane * 16;
  u4 b0, b1; float g;
#pragma unroll
  for (int p = 0; p < 4; p++) {
    if (p == 0) WAITV(24); else if (p == 1) WAITV(16); else if (p == 2) WAITV(8); else WAITV(0);
    BAR();
    if (p == 3) {
      b0 = ((const u4*)(ws + O_BP))[(tt * 32 + ft * 2) * 64 + lane];
      b1 = ((const u4*)(ws + O_BP))[(tt * 32 + ft * 2 + 1) * 64 + lane];
      g = ((const float*)(ws + O_G))[tt * 32 + l31];
    }
#pragma unroll
    for (int k = 0; k < 8; k++) acc = mfma(*(const u4*)(ap + (p * 8 + k) * 1024), *(const u4*)(bp + (p * 8 + k) * 1024), acc);
  }
  const float g1 = 1.0f - g;
#pragma unroll
  for (int s = 0; s < 2; s++) {
    const u4 bb = s ? b1 : b0;
    unsigned r[4];
#pragma unroll
    for (int i = 0; i < 4; i++) r[i] = pk(g * acc[8 * s + 2 * i] + g1 * blo(bb[i]), g * acc[8 * s + 2 * i + 1] + g1 * bhi(bb[i]));
    auto e0 = __builtin_amdgcn_permlane32_swap(r[0], r[2], false, false);
    auto e1 = __builtin_amdgcn_permlane32_swap(r[1], r[3], false, false);
    u4 v = {e0[0], e1[0], e0[1], e1[1]};
    *(u4*)(out + (tt * 32 + l31) * 512 + ft * 32 + 16 * s + 8 * h) = v;
  }
}

extern "C" void kernel_launch(void* const* d_in, const int* in_sizes, int n_in, void* d_out, int out_size, void* d_ws,
                              size_t ws_size, hipStream_t stream) {
  const u16* x = (const u16*)d_in[0];
  char* ws = (char*)d_ws;
  k1<<<256 + 288 + 28, 256, 0, stream>>>(x, (const u16*)d_in[1], (const u16*)d_in[2], (const u16*)d_in[4], (const u16*)d_in[5],
                                         (const u16*)d_in[6], (const u16*)d_in[7], (const u16*)d_in[8], ws);
  k2<<<256, 768, 0, stream>>>(x, ws);
  k3<<<256, 256, 0, stream>>>(ws, (u16*)d_out);
}
```

```cpp
#include <hip/hip_runtime.h>
#include <hip/hip_bf16.h>

typedef unsigned short u16;
typedef __bf16 bf8 __attribute__((ext_vector_type(8)));
typedef __bf16 bf2 __attribute__((ext_vector_type(2)));
typedef float f16v __attribute__((ext_vector_type(16)));
typedef float f4 __attribute__((ext_vector_type(4)));
typedef float f2 __attribute__((ext_vector_type(2)));
typedef unsigned u4 __attribute__((ext_vector_type(4)));
typedef unsigned u2 __attribute__((ext_vector_type(2)));
typedef short s4 __attribute__((ext_vector_type(4)));
#define LAS __attribute__((address_space(3)))
#define GAS __attribute__((address_space(1)))
#define DI __device__ __forceinline__

#define MB (1u << 20)
#define O_QP (0u)
#define O_KP (2u * MB)
#define O_VP (4u * MB)
#define O_HS (6u * MB)
#define O_X1 (8u * MB)
#define O_BP (10u * MB)
#define O_OP (12u * MB)
#define O_H2 (14u * MB)
#define O_W2 (21u * MB)
#define O_WO (21u * MB + 512u * 1024u)
#define O_W3 (22u * MB)
#define O_W4 (24u * MB)
#define O_ST (26u * MB)
#define O_CS (27u * MB)
#define O_G (27u * MB + 8192u)
#define O_KM (27u * MB + 16384u)
#define O_CN (27u * MB + 32768u)

DI f16v mfma(u4 a, u4 b, f16v c) {
  return __builtin_amdgcn_mfma_f32_32x32x16_bf16(__builtin_bit_cast(bf8, a), __builtin_bit_cast(bf8, b), c, 0, 0, 0);
}
DI unsigned pk(float a, float b) {
  f2 v = {a, b};
  return __builtin_bit_cast(unsigned, __builtin_convertvector(v, bf2));
}
DI float dot2(unsigned a, unsigned b, float c) {
  return __builtin_amdgcn_fdot2_f32_bf16(__builtin_bit_cast(bf2, a), __builtin_bit_cast(bf2, b), c, false);
}
DI float blo(unsigned v) { return __builtin_bit_cast(float, v << 16); }
DI float bhi(unsigned v) { return __builtin_bit_cast(float, v & 0xffff0000u); }
DI float gelu(float v) {
  float e = __builtin_amdgcn_exp2f(v * __builtin_fmaf(v * v, -0.1029432397f, -2.3022081985f));
  return v * __builtin_amdgcn_rcpf(1.0f + e);
}
DI float xor32(float v) { return __shfl_xor(v, 32); }
DI void glds(const void* base, unsigned voff, unsigned l) {
  unsigned keep;
  asm volatile("s_mov_b32 %0, m0\n\ts_mov_b32 m0, %3\n\ts_nop 0\n\tglobal_load_lds_dwordx4 %1, %2\n\ts_mov_b32 m0, %0"
               : "=&s"(keep) : "v"(voff), "s"(base), "s"(l) : "memory");
}
DI unsigned laddr(char* p) { return __builtin_amdgcn_readfirstlane((unsigned)(size_t)(LAS char*)p); }
#define WAITV(n) asm volatile("s_waitcnt vmcnt(" #n ")" ::: "memory")
#define BAR() do { asm volatile("s_waitcnt lgkmcnt(0)" ::: "memory"); __builtin_amdgcn_s_barrier(); asm volatile("" ::: "memory"); } while (0)
#define ONES 0x3f803f80u

#define STAMP1(k)
#define STAMP(k)
template <int MODE>
DI void k1_gemm(char* sm, const u16* X, const u16* W, int ldw, int nb, int tb, const u16* gate, char* ws) {
  const int tid = threadIdx.x, lane = tid & 63, wid = __builtin_amdgcn_readfirstlane(tid >> 6);
  const int wr = wid >> 1, wc = wid & 1, h = lane >> 5, l31 = lane & 31;
  const unsigned xs = ((32 * wid + (lane >> 2)) * 512 + (((lane & 3) ^ ((lane >> 4) & 3)) * 8)) * 2;
  const unsigned wsA = ((8 * wid + (lane >> 4)) * ldw + (((lane & 15) ^ (((lane >> 4) << 2) | ((2 * wid) & 3))) * 8)) * 2;
  const unsigned wsB = ((8 * wid + 4 + (lane >> 4)) * ldw + (((lane & 15) ^ (((lane >> 4) << 2) | ((2 * wid + 1) & 3))) * 8)) * 2;
  const unsigned xl = laddr(sm) + wid * 2048, wl = xl + 8192;
  if (MODE == 0 && wid == 0) glds(gate, lane * 16, laddr(sm) + 65536);
  const int rot = ((MODE + tb) & 3) * 4;
#define STAGE(kc) do { const int kk = ((kc) + rot) & 15; \
    glds(X + kk * 32, xs, xl + ((kc) & 3) * 16384); glds(X + 16 * 512 + kk * 32, xs, xl + ((kc) & 3) * 16384 + 1024); \
    glds(W + kk * 32 * ldw, wsA, wl + ((kc) & 3) * 16384); glds(W + kk * 32 * ldw, wsB, wl + ((kc) & 3) * 16384 + 1024); } while (0)
  STAGE(0); STAGE(1); STAGE(2); STAGE(3);
  const int g1 = (lane >> 4) & 1, q = (lane & 15) >> 2, p = lane & 3;
  int aoff[2][2], boff[2];
  const int pn = MODE == 3 ? wc : 0, pt = MODE == 3 ? wr : 0;
#pragma unroll
  for (int nt = 0; nt < 2; nt++)
#pragma unroll
    for (int e = 0; e < 2; e++)
      aoff[nt][e] = 8192 + 256 * (8 * h + 4 * e + q) + 16 * ((8 * wr + 4 * (nt ^ pn) + 2 * g1 + (p >> 1)) ^ ((q << 2) | (2 * h + e))) + 8 * (p & 1);
#pragma unroll
  for (int s = 0; s < 2; s++) boff[s] = (64 * wc + l31) * 64 + (((2 * s + h) ^ ((l31 >> 2) & 3)) * 16);
  f16v acc[2][2];
#pragma unroll
  for (int i = 0; i < 16; i++) { acc[0][0][i] = 0.f; acc[0][1][i] = 0.f; acc[1][0][i] = 0.f; acc[1][1][i] = 0.f; }
  float s1[2] = {0.f, 0.f}, s2[2] = {0.f, 0.f}, cs[2] = {0.f, 0.f};
  u4 fa[2][2], fb[2][2], fg[2];
  auto rd = [&](int kc, int s) {
    const char* sb = sm + (kc & 3) * 16384;
#pragma unroll
    for (int nt = 0; nt < 2; nt++) {
      s4 lo = __builtin_amdgcn_ds_read_tr16_b64_v4i16((LAS s4*)(sb + aoff[nt][0] + s * 4096));
      s4 hi = __builtin_amdgcn_ds_read_tr16_b64_v4i16((LAS s4*)(sb + aoff[nt][1] + s * 4096));
      u2 l2 = __builtin_bit_cast(u2, lo), h2 = __builtin_bit_cast(u2, hi);
      fa[s][nt] = (u4){l2[0], l2[1], h2[0], h2[1]};
      fb[s][nt] = *(const u4*)(sb + boff[s] + (nt ^ pt) * 2048);
    }
    if (MODE == 0) fg[s] = *(const u4*)(sm + 65536 + (((kc + rot) & 15) * 32 + s * 16 + h * 8) * 2);
  };
  auto mm = [&](int s) {
#pragma unroll
    for (int nt = 0; nt < 2; nt++)
#pragma unroll
      for (int tt = 0; tt < 2; tt++)
        acc[nt][tt] = (MODE == 2) ? mfma(fb[s][tt], fa[s][nt], acc[nt][tt]) : mfma(fa[s][nt], fb[s][tt], acc[nt][tt]);
    if (MODE == 3) {
#pragma unroll
      for (int i = 0; i < 4; i++) {
        s1[0] = dot2(fb[s][0][i], ONES, s1[0]); s2[0] = dot2(fb[s][0][i], fb[s][0][i], s2[0]); cs[0] = dot2(fa[s][0][i], ONES, cs[0]);
      }
#pragma unroll
      for (int i = 0; i < 4; i++) { __builtin_amdgcn_sched_group_barrier(8, 1, 0); __builtin_amdgcn_sched_group_barrier(2, 3, 0); }
    }
    if (MODE == 0) {
      if (s == wr) {
#pragma unroll
        for (int tt = 0; tt < 2; tt++)
#pragma unroll
          for (int i = 0; i < 4; i++) s1[tt] = dot2(fb[s][tt][i], fg[s][i], s1[tt]);
      }
    }
  };
  WAITV(12);
  BAR();
  STAMP1(1);
  rd(0, 0);
#define UNR 16
#pragma unroll UNR
  for (int kc = 0; kc < 16; kc++) {
    rd(kc, 1);
    __builtin_amdgcn_sched_barrier(0);
    mm(0);
    __builtin_amdgcn_sched_barrier(0);
    if (kc < 15) {
      if (kc < 13) WAITV(8); else WAITV(0);
      BAR();
      rd(kc + 1, 0);
      if (kc + 4 < 16) STAGE(kc + 4);
    }
    __builtin_amdgcn_sched_barrier(0);
    mm(1);
    __builtin_amdgcn_sched_barrier(0);
  }
#undef STAGE
  STAMP1(2);
  const int T0 = tb * 4 + wc * 2;
  if (MODE == 0 || MODE == 1) {
    u4* dst = (u4*)(ws + (MODE == 0 ? O_QP : O_KP));
    const float sc = MODE == 0 ? 0.25f * 1.4426950409f : 1.0f;
#pragma unroll
    for (int nt = 0; nt < 2; nt++)
#pragma unroll
      for (int hh = 0; hh < 2; hh++) {
        const int head = (nb & 3) * 8 + wr * 4 + nt * 2 + hh;
        float mx = 0.f;
#pragma unroll
        for (int tt = 0; tt < 2; tt++) {
          u4 v;
#pragma unroll
          for (int i = 0; i < 4; i++) v[i] = pk(acc[nt][tt][8 * hh + 2 * i] * sc, acc[nt][tt][8 * hh + 2 * i + 1] * sc);
          dst[(head * 64 + T0 + tt) * 64 + lane] = v;
          if (MODE == 1) {
            float n2 = dot2(v[0], v[0], dot2(v[1], v[1], dot2(v[2], v[2], dot2(v[3], v[3], 0.f))));
            mx = fmaxf(mx, n2 + xor32(n2));
          }
        }
        if (MODE == 1) {
#pragma unroll
          for (int o = 16; o >= 1; o >>= 1) mx = fmaxf(mx, __shfl_xor(mx, o));
          if (lane == 0) ((float*)(ws + O_KM))[head * 32 + tb * 2 + wc] = mx;
        }
      }
    if (MODE == 0 && nb == 0) {
      float* red = (float*)sm;
      BAR();
      s1[0] += xor32(s1[0]); s1[1] += xor32(s1[1]);
      red[wr * 128 + wc * 64 + lane] = h ? s1[1] : s1[0];
      BAR();
      if (tid < 128) {
        float z = red[tid] + red[128 + tid];
        ((float*)(ws + O_G))[tb * 128 + tid] = __builtin_amdgcn_rcpf(1.0f + __builtin_amdgcn_exp2f(-1.4426950409f * z));
      }
    }
  } else if (MODE == 2) {
    u4* dst = (u4*)(ws + O_VP);
#pragma unroll
    for (int nt = 0; nt < 2; nt++) {
      const int head = (nb & 3) * 8 + wr * 4 + nt * 2 + g1;
#pragma unroll
      for (int tt = 0; tt < 2; tt++)
#pragma unroll
        for (int s = 0; s < 2; s++) {
          u4 v;
#pragma unroll
          for (int i = 0; i < 4; i++) v[i] = pk(acc[nt][tt][8 * s + 2 * i], acc[nt][tt][8 * s + 2 * i + 1]);
          dst[(head * 64 + T0 + tt) * 64 + s * 32 + h * 16 + (lane & 15)] = v;
        }
    }
  } else {
    float* red = (float*)sm;
    BAR();
    red[wc * 64 + wr * 32 + l31] = s1[0] + xor32(s1[0]);
    red[128 + wc * 64 + wr * 32 + l31] = s2[0] + xor32(s2[0]);
    red[256 + wr * 64 + wc * 32 + l31] = cs[0] + xor32(cs[0]);
    BAR();
    const int grp = nb & 3;
    const int dl = grp == 0 ? 1 : grp == 1 ? -1 : grp == 2 ? 2 : -2;
    u4* dst = (u4*)(ws + O_HS);
#pragma unroll
    for (int tt = 0; tt < 2; tt++) {
      const int tl = wc * 64 + (tt ^ wr) * 32 + l31;
      const float mean = red[tl] * (1.f / 512.f);
      const float var = fmaxf(red[128 + tl] * (1.f / 512.f) - mean * mean, 0.f);
      const float rstd = __builtin_amdgcn_rsqf(var + 1e-5f);
      const int td = tb * 128 + tl + dl;
      const bool ok = (unsigned)td < 2048u;
      const int tw = td & 2047;
#pragma unroll
      for (int nt = 0; nt < 2; nt++)
#pragma unroll
        for (int s = 0; s < 2; s++) {
          u4 v;
#pragma unroll
          for (int jj = 0; jj < 2; jj++) {
            const int f = wr * 64 + (nt ^ wc) * 32 + 16 * s + 8 * jj + 4 * h;
            f4 c = *(const f4*)(red + 256 + f);
            float r[4];
#pragma unroll
            for (int i = 0; i < 4; i++) r[i] = gelu(rstd * (acc[nt][tt][8 * s + 4 * jj + i] - mean * c[i]));
            v[2 * jj] = ok ? pk(r[0], r[1]) : 0u;
            v[2 * jj + 1] = ok ? pk(r[2], r[3]) : 0u;
          }
          dst[((tw >> 5) * 32 + grp * 8 + wr * 4 + (nt ^ wc) * 2 + s) * 64 + h * 32 + (tw & 31)] = v;
        }
    }
  }
}

__global__ __launch_bounds__(256, 2) void k1(const u16* x, const u16* wqkv, const u16* wo, const u16* w1, const u16* w2,
                                             const u16* w3, const u16* w4, const u16* gate, char* ws) {
  __shared__ __attribute__((aligned(16))) char sm[65536 + 1024];
  const int b = blockIdx.x, tid = threadIdx.x;
  STAMP1(0);
  if (b < 256) {
    const int xcd = b & 7, i = b >> 3;
    const int md = i & 3, nn = md * 4 + (xcd & 3), tb = (xcd >> 2) * 8 + (i >> 2);
    const u16* X = x + tb * 128 * 512;
    if (md == 0) k1_gemm<0>(sm, X, wqkv + nn * 128, 1536, nn, tb, gate, ws);
    else if (md == 1) k1_gemm<1>(sm, X, wqkv + nn * 128, 1536, nn, tb, gate, ws);
    else if (md == 2) k1_gemm<2>(sm, X, wqkv + nn * 128, 1536, nn, tb, gate, ws);
    else k1_gemm<3>(sm, X, w1 + (nn - 12) * 128, 512, nn, tb, gate, ws);
  } else if (b >= 284) {
    int u = (b - 284) * 256 + tid;
    if (b == 284 && tid < 64) ((unsigned*)(ws + O_CN))[tid * 32] = 0u;
    const u16* src; u4* dst; int N, KS;
    if (u < 8192) { src = w2; dst = (u4*)(ws + O_W2); N = 512; KS = 32; }
    else if (u < 16384) { u -= 8192; src = wo; dst = (u4*)(ws + O_WO); N = 512; KS = 32; }
    else if (u < 45056) { u -= 16384; src = w3; dst = (u4*)(ws + O_W3); N = 1792; KS = 32; }
    else { u -= 45056; src = w4; dst = (u4*)(ws + O_W4); N = 512; KS = 112; }
    const int nq = N >> 2, r = u / nq, n0 = (u - r * nq) * 4, h = r & 1, ks = r >> 1;
    u2 v[8];
#pragma unroll
    for (int j = 0; j < 8; j++) v[j] = *(const u2*)(src + (16 * ks + 8 * (j >> 2) + 4 * h + (j & 3)) * N + n0);
    u4* d = dst + ((n0 >> 5) * KS + ks) * 64 + h * 32 + (n0 & 31);
#pragma unroll
    for (int c = 0; c < 4; c++) {
      u4 o;
#pragma unroll
      for (int dd = 0; dd < 4; dd++)
        o[dd] = __builtin_amdgcn_perm(v[2 * dd + 1][c >> 1], v[2 * dd][c >> 1], (c & 1) ? 0x07060302u : 0x05040100u);
      d[c] = o;
    }
  } else {
    const int cb = b - 256, cq = tid & 15, sl = tid >> 4;
    const u16* src = w3 + (sl * 32) * 1792 + cb * 64 + cq * 4;
    f4 a = {0.f, 0.f, 0.f, 0.f};
#pragma unroll
    for (int r = 0; r < 32; r++) {
      u2 v = *(const u2*)(src + r * 1792);
      a[0] += blo(v[0]); a[1] += bhi(v[0]); a[2] += blo(v[1]); a[3] += bhi(v[1]);
    }
    *(f4*)(sm + (sl * 64 + cq * 4) * 4) = a;
    __syncthreads();
    if (tid < 64) {
      float t = 0.f;
#pragma unroll
      for (int r = 0; r < 16; r++) t += ((const float*)sm)[r * 64 + tid];
      ((float*)(ws + O_CS))[cb * 64 + tid] = t;
    }
  }
  STAMP1(3);
}

typedef __amdgpu_buffer_rsrc_t rsrc_t;
#define BLD(off, so, aux) __builtin_amdgcn_raw_buffer_load_b128(rs, (off), (so), (aux))
#define SB() __builtin_amdgcn_sched_barrier(0)
template <int MT, int NT, int D, int AUX, class F>
DI void wgemm(f16v (&acc)[MT][NT], rsrc_t rs, int A, int sa, int B, int sb, int nks, F pre) {
  u4 a[D][MT], b[D][NT];
#pragma unroll
  for (int d = 0; d < D; d++)
#pragma unroll
    for (int m = 0; m < MT; m++) a[d][m] = BLD(A + m * sa + (d & 3) * 1024, (d >> 2) * 4096, 0);
  SB();
  pre();
  SB();
#pragma unroll
  for (int d = 0; d < D; d++) {
#pragma unroll
    for (int n = 0; n < NT; n++) b[d][n] = BLD(B + n * sb + (d & 3) * 1024, (d >> 2) * 4096, AUX);
    SB();
  }
#pragma unroll 1
  for (int kb = D; kb < nks; kb += D) {
#pragma unroll
    for (int d = 0; d < D; d++) {
#pragma unroll
      for (int m = 0; m < MT; m++)
#pragma unroll
        for (int n = 0; n < NT; n++) acc[m][n] = mfma(a[d][m], b[d][n], acc[m][n]);
      SB();
#pragma unroll
      for (int m = 0; m < MT; m++) a[d][m] = BLD(A + m * sa + (d & 3) * 1024, kb * 1024 + (d >> 2) * 4096, 0);
#pragma unroll
      for (int n = 0; n < NT; n++) b[d][n] = BLD(B + n * sb + (d & 3) * 1024, kb * 1024 + (d >> 2) * 4096, AUX);
      SB();
    }
  }
#pragma unroll
  for (int d = 0; d < D; d++)
#pragma unroll
    for (int m = 0; m < MT; m++)
#pragma unroll
      for (int n = 0; n < NT; n++) acc[m][n] = mfma(a[d][m], b[d][n], acc[m][n]);
}
DI unsigned lds_add(unsigned* c) { return __hip_atomic_fetch_add(c, 1u, __ATOMIC_RELAXED, __HIP_MEMORY_SCOPE_WORKGROUP); }
DI void lds_arrive(unsigned* c, int lane) {
  asm volatile("s_waitcnt vmcnt(0) lgkmcnt(0)" ::: "memory");
  if (lane == 0) lds_add(c);
}
DI void lds_wait(unsigned* c, unsigned n) {
  while (__hip_atomic_load(c, __ATOMIC_RELAXED, __HIP_MEMORY_SCOPE_WORKGROUP) < n) __builtin_amdgcn_s_sleep(1);
  asm volatile("" ::: "memory");
}
DI void publish(unsigned* lc, GAS unsigned* gc, int lane) {
  asm volatile("s_waitcnt vmcnt(0)" ::: "memory");
  if (lane == 0) {
    if (lds_add(lc) == 3u) __hip_atomic_fetch_add(gc, 1u, __ATOMIC_RELAXED, __HIP_MEMORY_SCOPE_AGENT);
  }
}
DI void consume(GAS unsigned* gc, unsigned n) {
  while (__hip_atomic_load(gc, __ATOMIC_RELAXED, __HIP_MEMORY_SCOPE_AGENT) < n) __builtin_amdgcn_s_sleep(2);
  __builtin_amdgcn_fence(__ATOMIC_ACQUIRE, "wavefront");
  asm volatile("" ::: "memory");
}
#define L_V 65536
#define L_C 131072
#define L_R 133120
#define L_N 149504

__global__ __launch_bounds__(768, 3) void k2(const u16* x, char* ws) {
  __shared__ __attribute__((aligned(16))) char sm[L_N + 64];
  const int b = blockIdx.x, tid = threadIdx.x, lane = tid & 63, wid = __builtin_amdgcn_readfirstlane(tid >> 6);
  const int h = lane >> 5, l31 = lane & 31;
  const int grp = (b & 7) * 4 + ((b >> 3) & 3), sub = b >> 5;
  unsigned* cn = (unsigned*)(sm + L_N);
  const int qt = sub * 8 + wid;
  u4 qf; float km;
  STAMP(0);
  if (wid < 8) {
    qf = ((const u4*)(ws + O_QP))[(grp * 64 + qt) * 64 + lane];
    km = ((const float*)(ws + O_KM))[grp * 32 + l31];
    asm volatile("" ::: "memory");
    const char* kg = ws + O_KP + grp * 65536 + wid * 2048;
    const unsigned lb = laddr(sm) + wid * 2048, vo = lane * 16;
#pragma unroll
    for (int p = 0; p < 4; p++) {
      glds(kg + p * 16384, vo, lb + p * 16384);
      glds(kg + p * 16384 + 1024, vo, lb + p * 16384 + 1024);
      glds(kg + 2 * MB + p * 16384, vo, lb + L_V + p * 16384);
      glds(kg + 2 * MB + p * 16384 + 1024, vo, lb + L_V + p * 16384 + 1024);
    }
  } else {
    const int t = tid - 512;
    if (t < 128) { const unsigned v = (t & 31) == 0 ? ONES : 0u; *(u4*)(sm + L_C + t * 16) = (u4){v, v, v, v}; }
    if (t < 16) cn[t] = 0u;
  }
  BAR();
  if (wid < 8) {
#pragma unroll
    for (int o = 16; o >= 1; o >>= 1) km = fmaxf(km, __shfl_xor(km, o));
    float qn = dot2(qf[0], qf[0], dot2(qf[1], qf[1], dot2(qf[2], qf[2], dot2(qf[3], qf[3], 0.f))));
    qn += xor32(qn);
    float m = __builtin_sqrtf(qn * km) * 1.001f;
    const bool real = l31 < 16;
    const int vst = real ? 2048 : 0;
    const int v0 = real ? L_V + h * 256 + l31 * 16 : L_C + ((l31 & 3) == 0 && l31 < 24 ? 0 : 16);
    asm volatile("s_waitcnt vmcnt(12)" ::: "memory");
    if (lane == 0) lds_add(cn);
    lds_wait(cn, 8);
    STAMP(1);
    f16v o;
#pragma unroll 1
    for (int att = 0; att < 2; att++) {
      f16v ci, sA, sB;
#pragma unroll
      for (int i = 0; i < 16; i++) { ci[i] = -m; o[i] = 0.f; }
      const char* kp = sm + lane * 16;
      const char* vp = sm + v0;
      sA = mfma(*(const u4*)kp, qf, ci);
      u4 k1f = *(const u4*)(kp + 1024);
#pragma unroll 1
      for (int t = 0; t < 64; t += 2) {
        if (t == 14 && att == 0) { lds_arrive(cn + 1, lane); lds_wait(cn + 1, 8); }
        u4 va = *(const u4*)vp, vb = *(const u4*)(vp + 512), vc = *(const u4*)(vp + 1024), vd = *(const u4*)(vp + 1536);
        u4 k2f = *(const u4*)(kp + 2048);
        sB = mfma(k1f, qf, ci);
        k1f = *(const u4*)(kp + 3072);
        u4 p0, p1;
#pragma unroll
        for (int i = 0; i < 4; i++) p0[i] = pk(__builtin_amdgcn_exp2f(sA[2 * i]), __builtin_amdgcn_exp2f(sA[2 * i + 1]));
        o = mfma(va, p0, o);
#pragma unroll
        for (int i = 0; i < 4; i++) p1[i] = pk(__builtin_amdgcn_exp2f(sA[8 + 2 * i]), __builtin_amdgcn_exp2f(sA[9 + 2 * i]));
        o = mfma(vb, p1, o);
        sA = mfma(k2f, qf, ci);
#pragma unroll
        for (int i = 0; i < 4; i++) p0[i] = pk(__builtin_amdgcn_exp2f(sB[2 * i]), __builtin_amdgcn_exp2f(sB[2 * i + 1]));
        o = mfma(vc, p0, o);
#pragma unroll
        for (int i = 0; i < 4; i++) p1[i] = pk(__builtin_amdgcn_exp2f(sB[8 + 2 * i]), __builtin_amdgcn_exp2f(sB[9 + 2 * i]));
        o = mfma(vd, p1, o);
        kp += 2048; vp += vst;
      }
      if (att == 1 || __all(o[8] >= 8.67e-19f && o[8] < 1e30f)) break;
      f16v z;
#pragma unroll
      for (int i = 0; i < 16; i++) z[i] = 0.f;
      float mx = -3.0e38f;
#pragma unroll 1
      for (int t = 0; t < 64; t++) {
        f16v s = mfma(*(const u4*)(sm + lane * 16 + t * 1024), qf, z);
#pragma unroll
        for (int i = 0; i < 16; i++) mx = fmaxf(mx, s[i]);
      }
      m = fmaxf(mx, xor32(mx));
    }
    STAMP(2);
    const float il = 1.0f / o[8];
    u4 r;
#pragma unroll
    for (int i = 0; i < 4; i++) r[i] = pk(o[2 * i] * il, o[2 * i + 1] * il);
    ((u4*)(ws + O_OP))[(qt * 32 + grp) * 64 + lane] = r;
  } else {
#define PRIO 3
    __builtin_amdgcn_s_setprio(PRIO);
    const int gw = wid - 8, mi = gw >> 1, ni = gw & 1;
    GAS unsigned* gc = (GAS unsigned*)(ws + O_CN) + grp * 64;
    const __amdgpu_buffer_rsrc_t rs = __builtin_amdgcn_make_buffer_rsrc((void*)ws, (short)0, 0x7fffffff, 0x00020000);
    const int ft = sub * 2 + mi, tt = grp * 2 + ni;
    {
      f16v acc[1][1];
      u2 xr[4];
#pragma unroll
      for (int q = 0; q < 4; q++) xr[q] = *(const u2*)(x + (tt * 32 + l31) * 512 + ft * 32 + 8 * q + 4 * h);
#pragma unroll
      for (int i = 0; i < 16; i++) acc[0][0][i] = 0.f;
      wgemm<1, 1, 8, 0>(acc, rs, O_W2 + ft * 32768 + lane * 16, 0, O_HS + tt * 32768 + lane * 16, 0, 32, [] {});
      float s1 = 0.f, s2 = 0.f;
#pragma unroll
      for (int s = 0; s < 2; s++) {
        u4 v;
#pragma unroll
        for (int i = 0; i < 4; i++) {
          const unsigned xv = xr[2 * s + (i >> 1)][i & 1];
          v[i] = pk(acc[0][0][8 * s + 2 * i] + blo(xv), acc[0][0][8 * s + 2 * i + 1] + bhi(xv));
          s1 = dot2(v[i], ONES, s1); s2 = dot2(v[i], v[i], s2);
        }
        __builtin_amdgcn_raw_buffer_store_b128(v, rs, O_X1 + ((tt * 32 + ft * 2 + s) * 64 + lane) * 16, 0, 16);
      }
      u2 st = {__builtin_bit_cast(unsigned, s1), __builtin_bit_cast(unsigned, s2)};
      __builtin_amdgcn_raw_buffer_store_b64(st, rs, O_ST + ((tt * 16 + ft) * 64 + lane) * 8, 0, 16);
      STAMP(1);
      publish(cn + 2, gc, lane);
      STAMP(2);
    }
    const int j = sub * 4 + gw;
    if (j < 28) {
      f16v acc[2][2];
#pragma unroll
      for (int i = 0; i < 16; i++) { acc[0][0][i] = 0.f; acc[0][1][i] = 0.f; acc[1][0][i] = 0.f; acc[1][1][i] = 0.f; }
      wgemm<2, 2, 4, 16>(acc, rs, O_W3 + j * 65536 + lane * 16, 32768, O_X1 + grp * 65536 + lane * 16, 32768, 32, [&] { consume(gc, 8); STAMP(3); });
      SB();
      float mean[2], rstd[2];
#pragma unroll
      for (int n = 0; n < 2; n++) {
        f2 t = {0.f, 0.f};
#pragma unroll
        for (int f = 0; f < 16; f++) t += __builtin_bit_cast(f2, __builtin_amdgcn_raw_buffer_load_b64(rs, O_ST + (((grp * 2 + n) * 16 + f) * 64 + lane) * 8, 0, 16));
        t[0] += xor32(t[0]); t[1] += xor32(t[1]);
        mean[n] = t[0] * (1.f / 512.f);
        rstd[n] = __builtin_amdgcn_rsqf(fmaxf(t[1] * (1.f / 512.f) - mean[n] * mean[n], 0.f) + 1e-5f);
      }
#pragma unroll
      for (int mm = 0; mm < 2; mm++)
#pragma unroll
        for (int s = 0; s < 2; s++) {
          const float* cp = (const float*)(ws + O_CS) + j * 64 + mm * 32 + 16 * s + 4 * h;
          const f4 c0 = *(const f4*)cp, c1 = *(const f4*)(cp + 8);
#pragma unroll
          for (int n = 0; n < 2; n++) {
            float r[8];
#pragma unroll
            for (int i = 0; i < 4; i++) {
              r[i] = gelu(rstd[n] * (acc[mm][n][8 * s + i] - mean[n] * c0[i]));
              r[4 + i] = gelu(rstd[n] * (acc[mm][n][8 * s + 4 + i] - mean[n] * c1[i]));
            }
            u4 v = {pk(r[0], r[1]), pk(r[2], r[3]), pk(r[4], r[5]), pk(r[6], r[7])};
            __builtin_amdgcn_raw_buffer_store_b128(v, rs, O_H2 + (((grp * 2 + n) * 112 + j * 4 + mm * 2 + s) * 64 + lane) * 16, 0, 16);
          }
        }
      STAMP(4);
      publish(cn + 3, gc + 32, lane);
    }
    {
      f16v acc[2][2];
#pragma unroll
      for (int i = 0; i < 16; i++) { acc[0][0][i] = 0.f; acc[0][1][i] = 0.f; acc[1][0][i] = 0.f; acc[1][1][i] = 0.f; }
      const u4 x0 = BLD(O_X1 + ((tt * 32 + ft * 2) * 64 + lane) * 16, 0, 16), x1 = BLD(O_X1 + ((tt * 32 + ft * 2 + 1) * 64 + lane) * 16, 0, 16);
      wgemm<2, 2, 4, 16>(acc, rs, O_W4 + (ft * 112 + gw * 28) * 1024 + lane * 16, (1 - 2 * mi) * 114688,
                         O_H2 + (tt * 112 + gw * 28) * 1024 + lane * 16, (1 - 2 * ni) * 114688, 28, [&] { consume(gc + 32, 7); STAMP(5); });
      STAMP(6);
#pragma unroll
      for (int r = 1; r < 4; r++) {
        f4* dp = (f4*)(sm + L_R + (r ^ gw) * 4096) + lane;
        const f16v a = acc[r >> 1][r & 1];
#pragma unroll
        for (int i = 0; i < 4; i++) dp[i * 64] = (f4){a[4 * i], a[4 * i + 1], a[4 * i + 2], a[4 * i + 3]};
        lds_arrive(cn + 4, lane); lds_wait(cn + 4, 8 * r - 4);
        const f4* sp = (const f4*)(sm + L_R + gw * 4096) + lane;
#pragma unroll
        for (int i = 0; i < 4; i++) {
          const f4 v = sp[i * 64];
          acc[0][0][4 * i] += v[0]; acc[0][0][4 * i + 1] += v[1]; acc[0][0][4 * i + 2] += v[2]; acc[0][0][4 * i + 3] += v[3];
        }
        if (r < 3) { lds_arrive(cn + 4, lane); lds_wait(cn + 4, 8 * r); }
      }
      u4 v, w;
#pragma unroll
      for (int i = 0; i < 4; i++) {
        v[i] = pk(acc[0][0][2 * i] + blo(x0[i]), acc[0][0][2 * i + 1] + bhi(x0[i]));
        w[i] = pk(acc[0][0][8 + 2 * i] + blo(x1[i]), acc[0][0][9 + 2 * i] + bhi(x1[i]));
      }
      ((u4*)(ws + O_BP))[(tt * 32 + ft * 2) * 64 + lane] = v;
      ((u4*)(ws + O_BP))[(tt * 32 + ft * 2 + 1) * 64 + lane] = w;
      STAMP(7);
    }
  }
}

__global__ __launch_bounds__(256, 1) void k3(const char* ws, u16* out) {
  __shared__ __attribute__((aligned(16))) char sm[131072];
  const int b = blockIdx.x, tid = threadIdx.x, lane = tid & 63, wid = __builtin_amdgcn_readfirstlane(tid >> 6);
  const int h = lane >> 5, l31 = lane & 31;
  const int grp = (b & 7) * 4 + ((b >> 3) & 3), sub = b >> 5;
  const int mi = wid >> 1, ni = wid & 1, ft = sub * 2 + mi, tt = grp * 2 + ni;
  const char* src = ws + (wid < 2 ? O_WO + (sub * 2 + wid) * 32768 : O_OP + (grp * 2 + wid - 2) * 32768);
  const u4 b0 = ((const u4*)(ws + O_BP))[(tt * 32 + ft * 2) * 64 + lane];
  const u4 b1 = ((const u4*)(ws + O_BP))[(tt * 32 + ft * 2 + 1) * 64 + lane];
  const float g = ((const float*)(ws + O_G))[tt * 32 + l31];
  asm volatile("" ::: "memory");
#pragma unroll
  for (int k = 0; k < 32; k++) glds(src + k * 1024, lane * 16, laddr(sm) + wid * 32768 + k * 1024);
  f16v acc;
#pragma unroll
  for (int i = 0; i < 16; i++) acc[i] = 0.f;
  const char* ap = sm + mi * 32768 + lane * 16;
  const char* bp = sm + 65536 + ni * 32768 + lane * 16;
#pragma unroll
  for (int p = 0; p < 4; p++) {
    if (p == 0) WAITV(24); else if (p == 1) WAITV(16); else if (p == 2) WAITV(8); else WAITV(0);
    BAR();
#pragma unroll
    for (int k = 0; k < 8; k++) acc = mfma(*(const u4*)(ap + (p * 8 + k) * 1024), *(const u4*)(bp + (p * 8 + k) * 1024), acc);
  }
  const float g1 = 1.0f - g;
#pragma unroll
  for (int s = 0; s < 2; s++) {
    const u4 bb = s ? b1 : b0;
    unsigned r[4];
#pragma unroll
    for (int i = 0; i < 4; i++) r[i] = pk(g * acc[8 * s + 2 * i] + g1 * blo(bb[i]), g * acc[8 * s + 2 * i + 1] + g1 * bhi(bb[i]));
    auto e0 = __builtin_amdgcn_permlane32_swap(r[0], r[2], false, false);
    auto e1 = __builtin_amdgcn_permlane32_swap(r[1], r[3], false, false);
    u4 v = {e0[0], e1[0], e0[1], e1[1]};
    *(u4*)(out + (tt * 32 + l31) * 512 + ft * 32 + 16 * s + 8 * h) = v;
  }
}

extern "C" void kernel_launch(void* const* d_in, const int* in_sizes, int n_in, void* d_out, int out_size, void* d_ws,
                              size_t ws_size, hipStream_t stream) {
  const u16* x = (const u16*)d_in[0];
  char* ws = (char*)d_ws;
  k1<<<256 + 288 + 28, 256, 0, stream>>>(x, (const u16*)d_in[1], (const u16*)d_in[2], (const u16*)d_in[4], (const u16*)d_in[5],
                                         (const u16*)d_in[6], (const u16*)d_in[7], (const u16*)d_in[8], ws);
  k2<<<256, 768, 0, stream>>>(x, ws);
  k3<<<256, 256, 0, stream>>>(ws, (u16*)d_out);
}
```

```cpp
#include <hip/hip_runtime.h>
#include <hip/hip_bf16.h>

typedef unsigned short u16;
typedef __bf16 bf8 __attribute__((ext_vector_type(8)));
typedef __bf16 bf2 __attribute__((ext_vector_type(2)));
typedef float f16v __attribute__((ext_vector_type(16)));
typedef float f4 __attribute__((ext_vector_type(4)));
typedef float f2 __attribute__((ext_vector_type(2)));
typedef unsigned u4 __attribute__((ext_vector_type(4)));
typedef unsigned u2 __attribute__((ext_vector_type(2)));
typedef short s4 __attribute__((ext_vector_type(4)));
#define LAS __attribute__((address_space(3)))
#define GAS __attribute__((address_space(1)))
#define DI __device__ __forceinline__

#define MB (1u << 20)
#define O_QP (0u)
#define O_KP (2u * MB)
#define O_VP (4u * MB)
#define O_HS (6u * MB)
#define O_X1 (8u * MB)
#define O_BP (10u * MB)
#define O_OP (12u * MB)
#define O_H2 (14u * MB)
#define O_W2 (21u * MB)
#define O_WO (21u * MB + 512u * 1024u)
#define O_W3 (22u * MB)
#define O_W4 (24u * MB)
#define O_ST (26u * MB)
#define O_CS (27u * MB)
#define O_G (27u * MB + 8192u)
#define O_KM (27u * MB + 16384u)
#define O_CN (27u * MB + 32768u)

DI f16v mfma(u4 a, u4 b, f16v c) {
  return __builtin_amdgcn_mfma_f32_32x32x16_bf16(__builtin_bit_cast(bf8, a), __builtin_bit_cast(bf8, b), c, 0, 0, 0);
}
DI unsigned pk(float a, float b) {
  f2 v = {a, b};
  return __builtin_bit_cast(unsigned, __builtin_convertvector(v, bf2));
}
DI float dot2(unsigned a, unsigned b, float c) {
  return __builtin_amdgcn_fdot2_f32_bf16(__builtin_bit_cast(bf2, a), __builtin_bit_cast(bf2, b), c, false);
}
DI float blo(unsigned v) { return __builtin_bit_cast(float, v << 16); }
DI float bhi(unsigned v) { return __builtin_bit_cast(float, v & 0xffff0000u); }
DI float gelu(float v) {
  float e = __builtin_amdgcn_exp2f(v * __builtin_fmaf(v * v, -0.1029432397f, -2.3022081985f));
  return v * __builtin_amdgcn_rcpf(1.0f + e);
}
DI float xor32(float v) { return __shfl_xor(v, 32); }
DI void glds(const void* base, unsigned voff, unsigned l) {
  unsigned keep;
  asm volatile("s_mov_b32 %0, m0\n\ts_mov_b32 m0, %3\n\ts_nop 0\n\tglobal_load_lds_dwordx4 %1, %2\n\ts_mov_b32 m0, %0"
               : "=&s"(keep) : "v"(voff), "s"(base), "s"(l) : "memory");
}
DI unsigned laddr(char* p) { return __builtin_amdgcn_readfirstlane((unsigned)(size_t)(LAS char*)p); }
#define WAITV(n) asm volatile("s_waitcnt vmcnt(" #n ")" ::: "memory")
#define BAR() do { asm volatile("s_waitcnt lgkmcnt(0)" ::: "memory"); __builtin_amdgcn_s_barrier(); asm volatile("" ::: "memory"); } while (0)
#define ONES 0x3f803f80u

template <int MODE>
DI void k1_gemm(char* sm, const u16* X, const u16* W, int ldw, int nb, int tb, const u16* gate, char* ws) {
  const int tid = threadIdx.x, lane = tid & 63, wid = __builtin_amdgcn_readfirstlane(tid >> 6);
  const int wr = wid >> 1, wc = wid & 1, h = lane >> 5, l31 = lane & 31;
  const unsigned xs = ((32 * wid + (lane >> 2)) * 512 + (((lane & 3) ^ ((lane >> 4) & 3)) * 8)) * 2;
  const unsigned wsA = ((8 * wid + (lane >> 4)) * ldw + (((lane & 15) ^ (((lane >> 4) << 2) | ((2 * wid) & 3))) * 8)) * 2;
  const unsigned wsB = ((8 * wid + 4 + (lane >> 4)) * ldw + (((lane & 15) ^ (((lane >> 4) << 2) | ((2 * wid + 1) & 3))) * 8)) * 2;
  const unsigned xl = laddr(sm) + wid * 2048, wl = xl + 8192;
  if (MODE == 0 && wid == 0) glds(gate, lane * 16, laddr(sm) + 65536);
  const int rot = 0;
#define STAGE(kc) do { const int kk = ((kc) + rot) & 15; \
    glds(X + kk * 32, xs, xl + ((kc) & 3) * 16384); glds(X + 16 * 512 + kk * 32, xs, xl + ((kc) & 3) * 16384 + 1024); \
    glds(W + kk * 32 * ldw, wsA, wl + ((kc) & 3) * 16384); glds(W + kk * 32 * ldw, wsB, wl + ((kc) & 3) * 16384 + 1024); } while (0)
  STAGE(0); STAGE(1); STAGE(2); STAGE(3);
  const int g1 = (lane >> 4) & 1, q = (lane & 15) >> 2, p = lane & 3;
  int aoff[2][2], boff[2];
  const int pn = MODE == 3 ? wc : 0, pt = MODE == 3 ? wr : 0;
#pragma unroll
  for (int nt = 0; nt < 2; nt++)
#pragma unroll
    for (int e = 0; e < 2; e++)
      aoff[nt][e] = 8192 + 256 * (8 * h + 4 * e + q) + 16 * ((8 * wr + 4 * (nt ^ pn) + 2 * g1 + (p >> 1)) ^ ((q << 2) | (2 * h + e))) + 8 * (p & 1);
#pragma unroll
  for (int s = 0; s < 2; s++) boff[s] = (64 * wc + l31) * 64 + (((2 * s + h) ^ ((l31 >> 2) & 3)) * 16);
  f16v acc[2][2];
#pragma unroll
  for (int i = 0; i < 16; i++) { acc[0][0][i] = 0.f; acc[0][1][i] = 0.f; acc[1][0][i] = 0.f; acc[1][1][i] = 0.f; }
  float s1[2] = {0.f, 0.f}, s2[2] = {0.f, 0.f}, cs[2] = {0.f, 0.f};
  u4 fa[2][2], fb[2][2], fg[2];
  auto rd = [&](int kc, int s) {
    const char* sb = sm + (kc & 3) * 16384;
#pragma unroll
    for (int nt = 0; nt < 2; nt++) {
      s4 lo = __builtin_amdgcn_ds_read_tr16_b64_v4i16((LAS s4*)(sb + aoff[nt][0] + s * 4096));
      s4 hi = __builtin_amdgcn_ds_read_tr16_b64_v4i16((LAS s4*)(sb + aoff[nt][1] + s * 4096));
      u2 l2 = __builtin_bit_cast(u2, lo), h2 = __builtin_bit_cast(u2, hi);
      fa[s][nt] = (u4){l2[0], l2[1], h2[0], h2[1]};
      fb[s][nt] = *(const u4*)(sb + boff[s] + (nt ^ pt) * 2048);
    }
    if (MODE == 0) fg[s] = *(const u4*)(sm + 65536 + (((kc + rot) & 15) * 32 + s * 16 + h * 8) * 2);
  };
  auto mm = [&](int s) {
#pragma unroll
    for (int nt = 0; nt < 2; nt++)
#pragma unroll
      for (int tt = 0; tt < 2; tt++)
        acc[nt][tt] = (MODE == 2) ? mfma(fb[s][tt], fa[s][nt], acc[nt][tt]) : mfma(fa[s][nt], fb[s][tt], acc[nt][tt]);
    if (MODE == 3) {
#pragma unroll
      for (int i = 0; i < 4; i++) {
        s1[0] = dot2(fb[s][0][i], ONES, s1[0]); s2[0] = dot2(fb[s][0][i], fb[s][0][i], s2[0]); cs[0] = dot2(fa[s][0][i], ONES, cs[0]);
      }
#pragma unroll
      for (int i = 0; i < 4; i++) { __builtin_amdgcn_sched_group_barrier(8, 1, 0); __builtin_amdgcn_sched_group_barrier(2, 3, 0); }
    }
    if (MODE == 0) {
      if (s == wr) {
#pragma unroll
        for (int tt = 0; tt < 2; tt++)
#pragma unroll
          for (int i = 0; i < 4; i++) s1[tt] = dot2(fb[s][tt][i], fg[s][i], s1[tt]);
      }
    }
  };
  WAITV(12);
  BAR();
  rd(0, 0);
#define UNR 16
#pragma unroll UNR
  for (int kc = 0; kc < 16; kc++) {
    rd(kc, 1);
    __builtin_amdgcn_sched_barrier(0);
    mm(0);
    __builtin_amdgcn_sched_barrier(0);
    if (kc < 15) {
      if (kc < 13) WAITV(8); else if (kc == 13) WAITV(4); else WAITV(0);
      BAR();
      rd(kc + 1, 0);
      if (kc + 4 < 16) STAGE(kc + 4);
    }
    __builtin_amdgcn_sched_barrier(0);
    mm(1);
    __builtin_amdgcn_sched_barrier(0);
  }
#undef STAGE
  const int T0 = tb * 4 + wc * 2;
  if (MODE == 0 || MODE == 1) {
    u4* dst = (u4*)(ws + (MODE == 0 ? O_QP : O_KP));
    const float sc = MODE == 0 ? 0.25f * 1.4426950409f : 1.0f;
#pragma unroll
    for (int nt = 0; nt < 2; nt++)
#pragma unroll
      for (int hh = 0; hh < 2; hh++) {
        const int head = (nb & 3) * 8 + wr * 4 + nt * 2 + hh;
        float mx = 0.f;
#pragma unroll
        for (int tt = 0; tt < 2; tt++) {
          u4 v;
#pragma unroll
          for (int i = 0; i < 4; i++) v[i] = pk(acc[nt][tt][8 * hh + 2 * i] * sc, acc[nt][tt][8 * hh + 2 * i + 1] * sc);
          dst[(head * 64 + T0 + tt) * 64 + lane] = v;
          if (MODE == 1) {
            float n2 = dot2(v[0], v[0], dot2(v[1], v[1], dot2(v[2], v[2], dot2(v[3], v[3], 0.f))));
            mx = fmaxf(mx, n2 + xor32(n2));
          }
        }
        if (MODE == 1) {
#pragma unroll
          for (int o = 16; o >= 1; o >>= 1) mx = fmaxf(mx, __shfl_xor(mx, o));
          if (lane == 0) ((float*)(ws + O_KM))[head * 32 + tb * 2 + wc] = mx;
        }
      }
    if (MODE == 0 && nb == 0) {
      float* red = (float*)sm;
      BAR();
      s1[0] += xor32(s1[0]); s1[1] += xor32(s1[1]);
      red[wr * 128 + wc * 64 + lane] = h ? s1[1] : s1[0];
      BAR();
      if (tid < 128) {
        float z = red[tid] + red[128 + tid];
        ((float*)(ws + O_G))[tb * 128 + tid] = __builtin_amdgcn_rcpf(1.0f + __builtin_amdgcn_exp2f(-1.4426950409f * z));
      }
    }
  } else if (MODE == 2) {
    u4* dst = (u4*)(ws + O_VP);
#pragma unroll
    for (int nt = 0; nt < 2; nt++) {
      const int head = (nb & 3) * 8 + wr * 4 + nt * 2 + g1;
#pragma unroll
      for (int tt = 0; tt < 2; tt++)
#pragma unroll
        for (int s = 0; s < 2; s++) {
          u4 v;
#pragma unroll
          for (int i = 0; i < 4; i++) v[i] = pk(acc[nt][tt][8 * s + 2 * i], acc[nt][tt][8 * s + 2 * i + 1]);
          dst[(head * 64 + T0 + tt) * 64 + s * 32 + h * 16 + (lane & 15)] = v;
        }
    }
  } else {
    float* red = (float*)sm;
    BAR();
    red[wc * 64 + wr * 32 + l31] = s1[0] + xor32(s1[0]);
    red[128 + wc * 64 + wr * 32 + l31] = s2[0] + xor32(s2[0]);
    red[256 + wr * 64 + wc * 32 + l31] = cs[0] + xor32(cs[0]);
    BAR();
    const int grp = nb & 3;
    const int dl = grp == 0 ? 1 : grp == 1 ? -1 : grp == 2 ? 2 : -2;
    u4* dst = (u4*)(ws + O_HS);
#pragma unroll
    for (int tt = 0; tt < 2; tt++) {
      const int tl = wc * 64 + (tt ^ wr) * 32 + l31;
      const float mean = red[tl] * (1.f / 512.f);
      const float var = fmaxf(red[128 + tl] * (1.f / 512.f) - mean * mean, 0.f);
      const float rstd = __builtin_amdgcn_rsqf(var + 1e-5f);
      const int td = tb * 128 + tl + dl;
      const bool ok = (unsigned)td < 2048u;
      const int tw = td & 2047;
#pragma unroll
      for (int nt = 0; nt < 2; nt++)
#pragma unroll
        for (int s = 0; s < 2; s++) {
          u4 v;
#pragma unroll
          for (int jj = 0; jj < 2; jj++) {
            const int f = wr * 64 + (nt ^ wc) * 32 + 16 * s + 8 * jj + 4 * h;
            f4 c = *(const f4*)(red + 256 + f);
            float r[4];
#pragma unroll
            for (int i = 0; i < 4; i++) r[i] = gelu(rstd * (acc[nt][tt][8 * s + 4 * jj + i] - mean * c[i]));
            v[2 * jj] = ok ? pk(r[0], r[1]) : 0u;
            v[2 * jj + 1] = ok ? pk(r[2], r[3]) : 0u;
          }
          dst[((tw >> 5) * 32 + grp * 8 + wr * 4 + (nt ^ wc) * 2 + s) * 64 + h * 32 + (tw & 31)] = v;
        }
    }
  }
}

__global__ __launch_bounds__(256, 2) void k1(const u16* x, const u16* wqkv, const u16* wo, const u16* w1, const u16* w2,
                                             const u16* w3, const u16* w4, const u16* gate, char* ws) {
  __shared__ __attribute__((aligned(16))) char sm[65536 + 1024];
  const int b = blockIdx.x, tid = threadIdx.x;
  if (b < 256) {
    const int xcd = b & 7, i = b >> 3;
    const int md = i & 3, nn = md * 4 + (xcd & 3), tb = (xcd >> 2) * 8 + (i >> 2);
    const u16* X = x + tb * 128 * 512;
    if (md == 0) k1_gemm<0>(sm, X, wqkv + nn * 128, 1536, nn, tb, gate, ws);
    else if (md == 1) k1_gemm<1>(sm, X, wqkv + nn * 128, 1536, nn, tb, gate, ws);
    else if (md == 2) k1_gemm<2>(sm, X, wqkv + nn * 128, 1536, nn, tb, gate, ws);
    else k1_gemm<3>(sm, X, w1 + (nn - 12) * 128, 512, nn, tb, gate, ws);
  } else if (b >= 284) {
    int u = (b - 284) * 256 + tid;
    if (b == 284 && tid < 64) ((unsigned*)(ws + O_CN))[tid * 32] = 0u;
    const u16* src; u4* dst; int N, KS;
    if (u < 8192) { src = w2; dst = (u4*)(ws + O_W2); N = 512; KS = 32; }
    else if (u < 16384) { u -= 8192; src = wo; dst = (u4*)(ws + O_WO); N = 512; KS = 32; }
    else if (u < 45056) { u -= 16384; src = w3; dst = (u4*)(ws + O_W3); N = 1792; KS = 32; }
    else { u -= 45056; src = w4; dst = (u4*)(ws + O_W4); N = 512; KS = 112; }
    const int nq = N >> 2, r = u / nq, n0 = (u - r * nq) * 4, h = r & 1, ks = r >> 1;
    u2 v[8];
#pragma unroll
    for (int j = 0; j < 8; j++) v[j] = *(const u2*)(src + (16 * ks + 8 * (j >> 2) + 4 * h + (j & 3)) * N + n0);
    u4* d = dst + ((n0 >> 5) * KS + ks) * 64 + h * 32 + (n0 & 31);
#pragma unroll
    for (int c = 0; c < 4; c++) {
      u4 o;
#pragma unroll
      for (int dd = 0; dd < 4; dd++)
        o[dd] = __builtin_amdgcn_perm(v[2 * dd + 1][c >> 1], v[2 * dd][c >> 1], (c & 1) ? 0x07060302u : 0x05040100u);
      d[c] = o;
    }
  } else {
    const int cb = b - 256, cq = tid & 15, sl = tid >> 4;
    const u16* src = w3 + (sl * 32) * 1792 + cb * 64 + cq * 4;
    f4 a = {0.f, 0.f, 0.f, 0.f};
#pragma unroll
    for (int r = 0; r < 32; r++) {
      u2 v = *(const u2*)(src + r * 1792);
      a[0] += blo(v[0]); a[1] += bhi(v[0]); a[2] += blo(v[1]); a[3] += bhi(v[1]);
    }
    *(f4*)(sm + (sl * 64 + cq * 4) * 4) = a;
    __syncthreads();
    if (tid < 64) {
      float t = 0.f;
#pragma unroll
      for (int r = 0; r < 16; r++) t += ((const float*)sm)[r * 64 + tid];
      ((float*)(ws + O_CS))[cb * 64 + tid] = t;
    }
  }
}

typedef __amdgpu_buffer_rsrc_t rsrc_t;
#define BLD(off, so, aux) __builtin_amdgcn_raw_buffer_load_b128(rs, (off), (so), (aux))
#define SB() __builtin_amdgcn_sched_barrier(0)
template <int MT, int NT, int D, int AUX, class F>
DI void wgemm(f16v (&acc)[MT][NT], rsrc_t rs, int A, int sa, int B, int sb, int nks, F pre) {
  u4 a[D][MT], b[D][NT];
#pragma unroll
  for (int d = 0; d < D; d++)
#pragma unroll
    for (int m = 0; m < MT; m++) a[d][m] = BLD(A + m * sa + (d & 3) * 1024, (d >> 2) * 4096, 0);
  SB();
  pre();
  SB();
#pragma unroll
  for (int d = 0; d < D; d++) {
#pragma unroll
    for (int n = 0; n < NT; n++) b[d][n] = BLD(B + n * sb + (d & 3) * 1024, (d >> 2) * 4096, AUX);
    SB();
  }
#pragma unroll 1
  for (int kb = D; kb < nks; kb += D) {
#pragma unroll
    for (int d = 0; d < D; d++) {
#pragma unroll
      for (int m = 0; m < MT; m++)
#pragma unroll
        for (int n = 0; n < NT; n++) acc[m][n] = mfma(a[d][m], b[d][n], acc[m][n]);
      SB();
#pragma unroll
      for (int m = 0; m < MT; m++) a[d][m] = BLD(A + m * sa + (d & 3) * 1024, kb * 1024 + (d >> 2) * 4096, 0);
#pragma unroll
      for (int n = 0; n < NT; n++) b[d][n] = BLD(B + n * sb + (d & 3) * 1024, kb * 1024 + (d >> 2) * 4096, AUX);
      SB();
    }
  }
#pragma unroll
  for (int d = 0; d < D; d++)
#pragma unroll
    for (int m = 0; m < MT; m++)
#pragma unroll
      for (int n = 0; n < NT; n++) acc[m][n] = mfma(a[d][m], b[d][n], acc[m][n]);
}
DI unsigned lds_add(unsigned* c) { return __hip_atomic_fetch_add(c, 1u, __ATOMIC_RELAXED, __HIP_MEMORY_SCOPE_WORKGROUP); }
DI void lds_arrive(unsigned* c, int lane) {
  asm volatile("s_waitcnt vmcnt(0) lgkmcnt(0)" ::: "memory");
  if (lane == 0) lds_add(c);
}
DI void lds_wait(unsigned* c, unsigned n) {
  while (__hip_atomic_load(c, __ATOMIC_RELAXED, __HIP_MEMORY_SCOPE_WORKGROUP) < n) __builtin_amdgcn_s_sleep(1);
  asm volatile("" ::: "memory");
}
DI void publish(unsigned* lc, GAS unsigned* gc, int lane) {
  asm volatile("s_waitcnt vmcnt(0)" ::: "memory");
  if (lane == 0) {
    if (lds_add(lc) == 3u) __hip_atomic_fetch_add(gc, 1u, __ATOMIC_RELAXED, __HIP_MEMORY_SCOPE_AGENT);
  }
}
DI void consume(GAS unsigned* gc, unsigned n) {
  while (__hip_atomic_load(gc, __ATOMIC_RELAXED, __HIP_MEMORY_SCOPE_AGENT) < n) __builtin_amdgcn_s_sleep(2);
  __builtin_amdgcn_fence(__ATOMIC_ACQUIRE, "wavefront");
  asm volatile("" ::: "memory");
}
#define L_V 65536
#define L_C 131072
#define L_R 133120
#define L_N 149504

__global__ __launch_bounds__(768, 3) void k2(const u16* x, char* ws) {
  __shared__ __attribute__((aligned(16))) char sm[L_N + 64];
  const int b = blockIdx.x, tid = threadIdx.x, lane = tid & 63, wid = __builtin_amdgcn_readfirstlane(tid >> 6);
  const int h = lane >> 5, l31 = lane & 31;
  const int grp = (b & 7) * 4 + ((b >> 3) & 3), sub = b >> 5;
  unsigned* cn = (unsigned*)(sm + L_N);
  const int qt = sub * 8 + wid;
  u4 qf; float km;
  if (wid < 8) {
    qf = ((const u4*)(ws + O_QP))[(grp * 64 + qt) * 64 + lane];
    km = ((const float*)(ws + O_KM))[grp * 32 + l31];
    asm volatile("" ::: "memory");
    const char* kg = ws + O_KP + grp * 65536 + wid * 2048;
    const unsigned lb = laddr(sm) + wid * 2048, vo = lane * 16;
#pragma unroll
    for (int p = 0; p < 4; p++) {
      glds(kg + p * 16384, vo, lb + p * 16384);
      glds(kg + p * 16384 + 1024, vo, lb + p * 16384 + 1024);
      glds(kg + 2 * MB + p * 16384, vo, lb + L_V + p * 16384);
      glds(kg + 2 * MB + p * 16384 + 1024, vo, lb + L_V + p * 16384 + 1024);
    }
  } else {
    const int t = tid - 512;
    if (t < 128) { const unsigned v = (t & 31) == 0 ? ONES : 0u; *(u4*)(sm + L_C + t * 16) = (u4){v, v, v, v}; }
    if (t < 16) cn[t] = 0u;
  }
  BAR();
  if (wid < 8) {
#pragma unroll
    for (int o = 16; o >= 1; o >>= 1) km = fmaxf(km, __shfl_xor(km, o));
    float qn = dot2(qf[0], qf[0], dot2(qf[1], qf[1], dot2(qf[2], qf[2], dot2(qf[3], qf[3], 0.f))));
    qn += xor32(qn);
    float m = __builtin_sqrtf(qn * km) * 1.001f;
    const bool real = l31 < 16;
    const int vst = real ? 2048 : 0;
    const int v0 = real ? L_V + h * 256 + l31 * 16 : L_C + ((l31 & 3) == 0 && l31 < 24 ? 0 : 16);
    asm volatile("s_waitcnt vmcnt(12)" ::: "memory");
    if (lane == 0) lds_add(cn);
    lds_wait(cn, 8);
    f16v o;
#pragma unroll 1
    for (int att = 0; att < 2; att++) {
      f16v ci, sA, sB;
#pragma unroll
      for (int i = 0; i < 16; i++) { ci[i] = -m; o[i] = 0.f; }
      const char* kp = sm + lane * 16;
      const char* vp = sm + v0;
      sA = mfma(*(const u4*)kp, qf, ci);
      u4 k1f = *(const u4*)(kp + 1024);
#pragma unroll 1
      for (int t = 0; t < 64; t += 2) {
        if (t == 14 && att == 0) { lds_arrive(cn + 1, lane); lds_wait(cn + 1, 8); }
        u4 va = *(const u4*)vp, vb = *(const u4*)(vp + 512), vc = *(const u4*)(vp + 1024), vd = *(const u4*)(vp + 1536);
        u4 k2f = *(const u4*)(kp + 2048);
        sB = mfma(k1f, qf, ci);
        k1f = *(const u4*)(kp + 3072);
        u4 p0, p1;
#pragma unroll
        for (int i = 0; i < 4; i++) p0[i] = pk(__builtin_amdgcn_exp2f(sA[2 * i]), __builtin_amdgcn_exp2f(sA[2 * i + 1]));
        o = mfma(va, p0, o);
#pragma unroll
        for (int i = 0; i < 4; i++) p1[i] = pk(__builtin_amdgcn_exp2f(sA[8 + 2 * i]), __builtin_amdgcn_exp2f(sA[9 + 2 * i]));
        o = mfma(vb, p1, o);
        sA = mfma(k2f, qf, ci);
#pragma unroll
        for (int i = 0; i < 4; i++) p0[i] = pk(__builtin_amdgcn_exp2f(sB[2 * i]), __builtin_amdgcn_exp2f(sB[2 * i + 1]));
        o = mfma(vc, p0, o);
#pragma unroll
        for (int i = 0; i < 4; i++) p1[i] = pk(__builtin_amdgcn_exp2f(sB[8 + 2 * i]), __builtin_amdgcn_exp2f(sB[9 + 2 * i]));
        o = mfma(vd, p1, o);
        kp += 2048; vp += vst;
      }
      if (att == 1 || __all(o[8] >= 8.67e-19f && o[8] < 1e30f)) break;
      f16v z;
#pragma unroll
      for (int i = 0; i < 16; i++) z[i] = 0.f;
      float mx = -3.0e38f;
#pragma unroll 1
      for (int t = 0; t < 64; t++) {
        f16v s = mfma(*(const u4*)(sm + lane * 16 + t * 1024), qf, z);
#pragma unroll
        for (int i = 0; i < 16; i++) mx = fmaxf(mx, s[i]);
      }
      m = fmaxf(mx, xor32(mx));
    }
    const float il = 1.0f / o[8];
    u4 r;
#pragma unroll
    for (int i = 0; i < 4; i++) r[i] = pk(o[2 * i] * il, o[2 * i + 1] * il);
    ((u4*)(ws + O_OP))[(qt * 32 + grp) * 64 + lane] = r;
  } else {
#define PRIO 3
    __builtin_amdgcn_s_setprio(PRIO);
    const int gw = wid - 8, mi = gw >> 1, ni = gw & 1;
    GAS unsigned* gc = (GAS unsigned*)(ws + O_CN) + grp * 64;
    const __amdgpu_buffer_rsrc_t rs = __builtin_amdgcn_make_buffer_rsrc((void*)ws, (short)0, 0x7fffffff, 0x00020000);
    const int ft = sub * 2 + mi, tt = grp * 2 + ni;
    {
      f16v acc[1][1];
      u2 xr[4];
#pragma unroll
      for (int q = 0; q < 4; q++) xr[q] = *(const u2*)(x + (tt * 32 + l31) * 512 + ft * 32 + 8 * q + 4 * h);
#pragma unroll
      for (int i = 0; i < 16; i++) acc[0][0][i] = 0.f;
      wgemm<1, 1, 8, 0>(acc, rs, O_W2 + ft * 32768 + lane * 16, 0, O_HS + tt * 32768 + lane * 16, 0, 32, [] {});
      float s1 = 0.f, s2 = 0.f;
#pragma unroll
      for (int s = 0; s < 2; s++) {
        u4 v;
#pragma unroll
        for (int i = 0; i < 4; i++) {
          const unsigned xv = xr[2 * s + (i >> 1)][i & 1];
          v[i] = pk(acc[0][0][8 * s + 2 * i] + blo(xv), acc[0][0][8 * s + 2 * i + 1] + bhi(xv));
          s1 = dot2(v[i], ONES, s1); s2 = dot2(v[i], v[i], s2);
        }
        __builtin_amdgcn_raw_buffer_store_b128(v, rs, O_X1 + ((tt * 32 + ft * 2 + s) * 64 + lane) * 16, 0, 16);
      }
      u2 st = {__builtin_bit_cast(unsigned, s1), __builtin_bit_cast(unsigned, s2)};
      __builtin_amdgcn_raw_buffer_store_b64(st, rs, O_ST + ((tt * 16 + ft) * 64 + lane) * 8, 0, 16);
      publish(cn + 2, gc, lane);
    }
    const int j = sub * 4 + gw;
    if (j < 28) {
      f16v acc[2][2];
#pragma unroll
      for (int i = 0; i < 16; i++) { acc[0][0][i] = 0.f; acc[0][1][i] = 0.f; acc[1][0][i] = 0.f; acc[1][1][i] = 0.f; }
      wgemm<2, 2, 4, 16>(acc, rs, O_W3 + j * 65536 + lane * 16, 32768, O_X1 + grp * 65536 + lane * 16, 32768, 32, [&] { consume(gc, 8); });
      SB();
      float mean[2], rstd[2];
#pragma unroll
      for (int n = 0; n < 2; n++) {
        f2 t = {0.f, 0.f};
#pragma unroll
        for (int f = 0; f < 16; f++) t += __builtin_bit_cast(f2, __builtin_amdgcn_raw_buffer_load_b64(rs, O_ST + (((grp * 2 + n) * 16 + f) * 64 + lane) * 8, 0, 16));
        t[0] += xor32(t[0]); t[1] += xor32(t[1]);
        mean[n] = t[0] * (1.f / 512.f);
        rstd[n] = __builtin_amdgcn_rsqf(fmaxf(t[1] * (1.f / 512.f) - mean[n] * mean[n], 0.f) + 1e-5f);
      }
#pragma unroll
      for (int mm = 0; mm < 2; mm++)
#pragma unroll
        for (int s = 0; s < 2; s++) {
          const float* cp = (const float*)(ws + O_CS) + j * 64 + mm * 32 + 16 * s + 4 * h;
          const f4 c0 = *(const f4*)cp, c1 = *(const f4*)(cp + 8);
#pragma unroll
          for (int n = 0; n < 2; n++) {
            float r[8];
#pragma unroll
            for (int i = 0; i < 4; i++) {
              r[i] = gelu(rstd[n] * (acc[mm][n][8 * s + i] - mean[n] * c0[i]));
              r[4 + i] = gelu(rstd[n] * (acc[mm][n][8 * s + 4 + i] - mean[n] * c1[i]));
            }
            u4 v = {pk(r[0], r[1]), pk(r[2], r[3]), pk(r[4], r[5]), pk(r[6], r[7])};
            __builtin_amdgcn_raw_buffer_store_b128(v, rs, O_H2 + (((grp * 2 + n) * 112 + j * 4 + mm * 2 + s) * 64 + lane) * 16, 0, 16);
          }
        }
      publish(cn + 3, gc + 32, lane);
    }
    {
      f16v acc[2][2];
#pragma unroll
      for (int i = 0; i < 16; i++) { acc[0][0][i] = 0.f; acc[0][1][i] = 0.f; acc[1][0][i] = 0.f; acc[1][1][i] = 0.f; }
      const u4 x0 = BLD(O_X1 + ((tt * 32 + ft * 2) * 64 + lane) * 16, 0, 16), x1 = BLD(O_X1 + ((tt * 32 + ft * 2 + 1) * 64 + lane) * 16, 0, 16);
      wgemm<2, 2, 4, 16>(acc, rs, O_W4 + (ft * 112 + gw * 28) * 1024 + lane * 16, (1 - 2 * mi) * 114688,
                         O_H2 + (tt * 112 + gw * 28) * 1024 + lane * 16, (1 - 2 * ni) * 114688, 28, [&] { consume(gc + 32, 7); });
#pragma unroll
      for (int r = 1; r < 4; r++) {
        f4* dp = (f4*)(sm + L_R + (r ^ gw) * 4096) + lane;
        const f16v a = acc[r >> 1][r & 1];
#pragma unroll
        for (int i = 0; i < 4; i++) dp[i * 64] = (f4){a[4 * i], a[4 * i + 1], a[4 * i + 2], a[4 * i + 3]};
        lds_arrive(cn + 4, lane); lds_wait(cn + 4, 8 * r - 4);
        const f4* sp = (const f4*)(sm + L_R + gw * 4096) + lane;
#pragma unroll
        for (int i = 0; i < 4; i++) {
          const f4 v = sp[i * 64];
          acc[0][0][4 * i] += v[0]; acc[0][0][4 * i + 1] += v[1]; acc[0][0][4 * i + 2] += v[2]; acc[0][0][4 * i + 3] += v[3];
        }
        if (r < 3) { lds_arrive(cn + 4, lane); lds_wait(cn + 4, 8 * r); }
      }
      u4 v, w;
#pragma unroll
      for (int i = 0; i < 4; i++) {
        v[i] = pk(acc[0][0][2 * i] + blo(x0[i]), acc[0][0][2 * i + 1] + bhi(x0[i]));
        w[i] = pk(acc[0][0][8 + 2 * i] + blo(x1[i]), acc[0][0][9 + 2 * i] + bhi(x1[i]));
      }
      ((u4*)(ws + O_BP))[(tt * 32 + ft * 2) * 64 + lane] = v;
      ((u4*)(ws + O_BP))[(tt * 32 + ft * 2 + 1) * 64 + lane] = w;
    }
  }
}

__global__ __launch_bounds__(256, 1) void k3(const char* ws, u16* out) {
  __shared__ __attribute__((aligned(16))) char sm[131072];
  const int b = blockIdx.x, tid = threadIdx.x, lane = tid & 63, wid = __builtin_amdgcn_readfirstlane(tid >> 6);
  const int h = lane >> 5, l31 = lane & 31;
  const int grp = (b & 7) * 4 + ((b >> 3) & 3), sub = b >> 5;
  const int mi = wid >> 1, ni = wid & 1, ft = sub * 2 + mi, tt = grp * 2 + ni;
  const char* src = ws + (wid < 2 ? O_WO + (sub * 2 + wid) * 32768 : O_OP + (grp * 2 + wid - 2) * 32768);
  const u4 b0 = ((const u4*)(ws + O_BP))[(tt * 32 + ft * 2) * 64 + lane];
  const u4 b1 = ((const u4*)(ws + O_BP))[(tt * 32 + ft * 2 + 1) * 64 + lane];
  const float g = ((const float*)(ws + O_G))[tt * 32 + l31];
  asm volatile("" ::: "memory");
#pragma unroll
  for (int k = 0; k < 32; k++) glds(src + k * 1024, lane * 16, laddr(sm) + wid * 32768 + k * 1024);
  f16v acc;
#pragma unroll
  for (int i = 0; i < 16; i++) acc[i] = 0.f;
  const char* ap = sm + mi * 32768 + lane * 16;
  const char* bp = sm + 65536 + ni * 32768 + lane * 16;
#pragma unroll
  for (int p = 0; p < 4; p++) {
    if (p == 0) WAITV(24); else if (p == 1) WAITV(16); else if (p == 2) WAITV(8); else WAITV(0);
    BAR();
#pragma unroll
    for (int k = 0; k < 8; k++) acc = mfma(*(const u4*)(ap + (p * 8 + k) * 1024), *(const u4*)(bp + (p * 8 + k) * 1024), acc);
  }
  const float g1 = 1.0f - g;
#pragma unroll
  for (int s = 0; s < 2; s++) {
    const u4 bb = s ? b1 : b0;
    unsigned r[4];
#pragma unroll
    for (int i = 0; i < 4; i++) r[i] = pk(g * acc[8 * s + 2 * i] + g1 * blo(bb[i]), g * acc[8 * s + 2 * i + 1] + g1 * bhi(bb[i]));
    auto e0 = __builtin_amdgcn_permlane32_swap(r[0], r[2], false, false);
    auto e1 = __builtin_amdgcn_permlane32_swap(r[1], r[3], false, false);
    u4 v = {e0[0], e1[0], e0[1], e1[1]};
    *(u4*)(out + (tt * 32 + l31) * 512 + ft * 32 + 16 * s + 8 * h) = v;
  }
}

extern "C" void kernel_launch(void* const* d_in, const int* in_sizes, int n_in, void* d_out, int out_size, void* d_ws,
                              size_t ws_size, hipStream_t stream) {
  const u16* x = (const u16*)d_in[0];
  char* ws = (char*)d_ws;
  k1<<<256 + 288 + 28, 256, 0, stream>>>(x, (const u16*)d_in[1], (const u16*)d_in[2], (const u16*)d_in[4], (const u16*)d_in[5],
                                         (const u16*)d_in[6], (const u16*)d_in[7], (const u16*)d_in[8], ws);
  k2<<<256, 768, 0, stream>>>(x, ws);
  k3<<<256, 256, 0, stream>>>(ws, (u16*)d_out);
}
```
